```python
import torch

def _softmax(x, axis=-1):
    return torch.softmax(x, dim=axis)
def _gelu(x):
    return torch.nn.functional.gelu(x, approximate="tanh")
def _silu(x):
    return torch.nn.functional.silu(x)
def _sigmoid(x):
    return torch.sigmoid(x)
def _layernorm(x, axis=-1, eps=1e-5):
    m = x.mean(dim=axis, keepdim=True)
    v = x.var(dim=axis, keepdim=True, unbiased=False)
    return (x - m) / torch.sqrt(v + eps)
def _scatter_add(tgt, idx, src, axis=0):
    return tgt.clone().index_add_(axis, idx, src)
def _segment_sum(src, seg, n):
    out = torch.zeros((n,) + src.shape[1:], dtype=src.dtype)
    return out.index_add_(0, seg, src)
def _pad(x, pw):
    flat = tuple(v for pair in reversed(pw) for v in pair)
    return torch.nn.functional.pad(x, flat)

kernel_name = 'concat_heads__gqa_block_cached__gqa_block_cached__near_ridge'

def setup_inputs():

    _w = (lambda a: (lambda t: t.to(torch.bfloat16) if t.is_floating_point() else t)(torch.as_tensor(a)))
    def _A_setup():
        B, Sq, D = 32, 64, 576
        Hq, Hkv, Sk, Dh = 32, 16, 512, 128
        assert Hq % Hkv == 0, (Hq, Hkv)
        return {
            "x": _w((lambda *s: torch.randn(*s, dtype=torch.bfloat16) * 0.8)(B, Sq, D)),
            "k_cache": _w((lambda *s: torch.randn(*s, dtype=torch.bfloat16) * 0.8)(B, Hkv, Sk, Dh)),
            "v_cache": _w((lambda *s: torch.randn(*s, dtype=torch.bfloat16) * 0.05)(B, Hkv, Sk, Dh)),
            "wq": _w((lambda *s: torch.randn(*s, dtype=torch.bfloat16) * 0.8)(D, Hq * Dh) * (D ** -0.5)),
            "wo": _w((lambda *s: torch.randn(*s, dtype=torch.bfloat16) * 0.05)(Hq * Dh, D) * ((Hq * Dh) ** -0.5)),
        }
    _A_in = _A_setup()

    def _B_setup():
        B, Sq, D = 32, 64, 576
        Hq, Hkv, Sk, Dh = 32, 16, 512, 128
        assert Hq % Hkv == 0, (Hq, Hkv)
        return {
            "x": _w((lambda *s: torch.randn(*s, dtype=torch.bfloat16) * 0.8)(B, Sq, D)),
            "k_cache": _w((lambda *s: torch.randn(*s, dtype=torch.bfloat16) * 0.8)(B, Hkv, Sk, Dh)),
            "v_cache": _w((lambda *s: torch.randn(*s, dtype=torch.bfloat16) * 0.05)(B, Hkv, Sk, Dh)),
            "wq": _w((lambda *s: torch.randn(*s, dtype=torch.bfloat16) * 0.8)(D, Hq * Dh) * (D ** -0.5)),
            "wo": _w((lambda *s: torch.randn(*s, dtype=torch.bfloat16) * 0.05)(Hq * Dh, D) * ((Hq * Dh) ** -0.5)),
        }
    _B_in = _B_setup()

    return {
        "A_x": _A_in["x"],
        "A_k_cache": _A_in["k_cache"],
        "A_v_cache": _A_in["v_cache"],
        "A_wq": _A_in["wq"],
        "A_wo": _A_in["wo"],
        "B_x": _B_in["x"],
        "B_k_cache": _B_in["k_cache"],
        "B_v_cache": _B_in["v_cache"],
        "B_wq": _B_in["wq"],
        "B_wo": _B_in["wo"],
    }

def reference(A_x, A_k_cache, A_v_cache, A_wq, A_wo, B_x, B_k_cache, B_v_cache, B_wq, B_wo):
    def _A_ref(x, k_cache, v_cache, wq, wo):
        B, Sq, D = x.shape
        Hq, Dh = 32, 128
        Hkv = k_cache.shape[1]
        rep = Hq // Hkv
        q = (lambda x, *d: x.permute(*d))((x @ wq).reshape(B, Sq, Hq, Dh), 0, 2, 1, 3)
        k = (lambda x, n, axis: torch.repeat_interleave(x, n, dim=axis))(k_cache, rep, axis=1)
        v = (lambda x, n, axis: torch.repeat_interleave(x, n, dim=axis))(v_cache, rep, axis=1)
        scores = torch.einsum("bhqd,bhkd->bhqk", q, k) * (Dh ** -0.5)
        p = _softmax(scores, axis=-1)
        o = torch.einsum("bhqk,bhkd->bhqd", p, v)
        return (lambda x, *d: x.permute(*d))(o, 0, 2, 1, 3).reshape(B, Sq, Hq * Dh) @ wo

    def _B_ref(x, k_cache, v_cache, wq, wo):
        B, Sq, D = x.shape
        Hq, Dh = 32, 128
        Hkv = k_cache.shape[1]
        rep = Hq // Hkv
        q = (lambda x, *d: x.permute(*d))((x @ wq).reshape(B, Sq, Hq, Dh), 0, 2, 1, 3)
        k = (lambda x, n, axis: torch.repeat_interleave(x, n, dim=axis))(k_cache, rep, axis=1)
        v = (lambda x, n, axis: torch.repeat_interleave(x, n, dim=axis))(v_cache, rep, axis=1)
        scores = torch.einsum("bhqd,bhkd->bhqk", q, k) * (Dh ** -0.5)
        p = _softmax(scores, axis=-1)
        o = torch.einsum("bhqk,bhkd->bhqd", p, v)
        return (lambda x, *d: x.permute(*d))(o, 0, 2, 1, 3).reshape(B, Sq, Hq * Dh) @ wo

    a = _A_ref(A_x, A_k_cache, A_v_cache, A_wq, A_wo)
    b = _B_ref(B_x, B_k_cache, B_v_cache, B_wq, B_wo)
    return torch.concatenate([a, b], axis=-1)
```

```cpp
#include <hip/hip_runtime.h>
#include <hip/hip_bf16.h>
#include <stdint.h>

typedef __attribute__((ext_vector_type(4))) short s16x4;
typedef __attribute__((ext_vector_type(8))) short bf16x8;
typedef __attribute__((ext_vector_type(4))) float f32x4;
typedef __attribute__((ext_vector_type(16))) float f32x16;
typedef __attribute__((ext_vector_type(4))) unsigned u32x4;
typedef __attribute__((ext_vector_type(2))) unsigned u32x2;
#define LAS __attribute__((address_space(3)))
#define GAS __attribute__((address_space(1)))
#define DEVI __device__ __forceinline__

constexpr unsigned L_H = 0;
constexpr unsigned L_KV = 32768;
constexpr unsigned L_X0 = 81920;
constexpr unsigned L_W = 90112;
constexpr unsigned L_TOT = 163840;
constexpr size_t WS_SLAB = 0;
constexpr size_t WS_SLAB_BYTES = (size_t)256 * 3 * 4 * 9 * 1024;
constexpr size_t WS_FLAG = (size_t)32 << 20;
constexpr size_t WS_FLAG_BYTES = (size_t)256 * 4 * 64;

constexpr float SL2E = 0.088388347648318447f * 1.4426950408889634f;

DEVI bf16x8 lds128(unsigned a) { return *(LAS bf16x8*)(a); }
DEVI void st128(unsigned a, bf16x8 v) { *(LAS bf16x8*)(a) = v; }
DEVI bf16x8 tr2(unsigned a0, unsigned a1) {
  s16x4 lo = __builtin_amdgcn_ds_read_tr16_b64_v4i16((LAS s16x4*)(a0));
  s16x4 hi = __builtin_amdgcn_ds_read_tr16_b64_v4i16((LAS s16x4*)(a1));
  return (bf16x8){lo[0], lo[1], lo[2], lo[3], hi[0], hi[1], hi[2], hi[3]};
}
DEVI unsigned cvtpk(float lo, float hi) { unsigned r; asm("v_cvt_pk_bf16_f32 %0, %1, %2" : "=v"(r) : "v"(lo), "v"(hi)); return r; }
template <int B> DEVI bf16x8 pack8(const f32x16& v) {
  u32x4 w = {cvtpk(v[B], v[B + 1]), cvtpk(v[B + 2], v[B + 3]), cvtpk(v[B + 4], v[B + 5]), cvtpk(v[B + 6], v[B + 7])};
  return __builtin_bit_cast(bf16x8, w);
}
template <int OFF> DEVI void dma_nt(unsigned lds, const char* sb, unsigned voff) {
  asm volatile("s_mov_b32 m0, %0\n\ts_nop 0\n\tglobal_load_lds_dwordx4 %1, %2 offset:%3 nt" ::"s"(lds), "v"(voff), "s"(sb), "i"(OFF) : "memory");
}
DEVI void dma(unsigned lds, const char* sb, unsigned voff) {
  asm volatile("s_mov_b32 m0, %0\n\ts_nop 0\n\tglobal_load_lds_dwordx4 %1, %2" ::"s"(lds), "v"(voff), "s"(sb) : "memory");
}
#define WAITV(n) asm volatile("s_waitcnt vmcnt(" #n ")" ::: "memory")
#define WAITL0() asm volatile("s_waitcnt lgkmcnt(0)" ::: "memory")
#define BAR() asm volatile("s_waitcnt lgkmcnt(0)\n\ts_barrier" ::: "memory")
#define SBAR() __builtin_amdgcn_sched_barrier(0)
#define MFMA32(a, b, c) __builtin_amdgcn_mfma_f32_32x32x16_bf16(a, b, c, 0, 0, 0)
#define MFMA16(a, b, c) __builtin_amdgcn_mfma_f32_16x16x32_bf16(a, b, c, 0, 0, 0)

DEVI int xblk(int r0, int unit, int s) { int v = r0 + 9 - unit + s; v -= (v >= 18) ? 18 : 0; v -= (v >= 9) ? 9 : 0; return v; }

__global__ __launch_bounds__(512, 2) void fused_gqa(const char* __restrict__ xA, const char* __restrict__ kA, const char* __restrict__ vA,
                                                    const char* __restrict__ wqA, const char* __restrict__ woA, const char* __restrict__ xB,
                                                    const char* __restrict__ kB, const char* __restrict__ vB, const char* __restrict__ wqB,
                                                    const char* __restrict__ woB, char* __restrict__ out, char* __restrict__ ws) {
  extern __shared__ __attribute__((aligned(16))) char shm[];
  const int bid = blockIdx.x, br = (bid >> 2) & 1, g = bid & 3, b = bid >> 3;
  const int wid = __builtin_amdgcn_readfirstlane(threadIdx.x >> 6);
  const int lane = threadIdx.x & 63;
  const int rot = b & 3, r0 = b >> 2;
  const int r = lane & 31, h = lane >> 5, i16 = lane & 15, q4 = i16 >> 2, p4 = i16 & 3, pp = ((p4 & 1) << 1) | (p4 >> 1), gi = (lane >> 4) & 1,
            g4 = lane >> 4;

  if (wid < 4) {
    const int w = wid;
    const char* X = (br ? xB : xA) + (size_t)(b * 64 + 16 * w) * 1152;
    const char* Kc = (br ? kB : kA) + (size_t)b * 16 * 131072 + w * 4096;
    const char* Vc = (br ? vB : vA) + (size_t)b * 16 * 131072 + w * 4096;
    const unsigned laneK = r * 256 + ((h ^ (r & 15)) * 16);
    const unsigned laneV = (4 * h + q4) * 256 + q4 * 64 + gi * 32 + pp * 8;
    const unsigned voffK = g4 * 256 + ((i16 ^ g4) * 16);
    const unsigned voffV = g4 * 256 + ((i16 ^ (g4 << 2)) * 16);
    const unsigned voffX = (lane >> 3) * 1152 + (((lane & 7) ^ g4) * 16);
    auto issueK = [&](int unit, int tile, unsigned slot) {
      const char* s = Kc + (size_t)(4 * g + ((unit + rot) & 3)) * 131072 + tile * 16384;
      const unsigned d = L_KV + slot * 16384 + w * 4096;
      dma_nt<0>(d, s, voffK); dma_nt<1024>(d, s, voffK ^ 64); dma_nt<2048>(d, s, voffK ^ 128); dma_nt<3072>(d, s, voffK ^ 192);
    };
    auto issueV = [&](int unit, int tile, unsigned slot) {
      const char* s = Vc + (size_t)(4 * g + ((unit + rot) & 3)) * 131072 + tile * 16384;
      const unsigned d = L_KV + slot * 16384 + w * 4096;
      dma_nt<0>(d, s, voffV); dma_nt<1024>(d, s, voffV); dma_nt<2048>(d, s, voffV); dma_nt<3072>(d, s, voffV);
    };
    auto issueX = [&](int unit, int s, unsigned dst) {
      const char* p = X + xblk(r0, unit, s) * 128;
      dma(dst + w * 2048, p, voffX); dma(dst + w * 2048 + 1024, p + 9216, voffX ^ 64);
    };
    issueX(0, 0, L_X0); issueX(0, 1, L_KV + 2 * 16384);
    issueX(0, 2, L_H); issueX(0, 3, L_H + 8192); issueX(0, 4, L_H + 16384); issueX(0, 5, L_H + 24576);
    issueK(0, 0, 0); issueV(0, 0, 1);
    WAITV(18); BAR();
    WAITV(16); BAR();
    WAITV(8); BAR();
    issueX(0, 8, L_X0); BAR();
    issueX(0, 6, L_H); BAR();
    issueX(0, 7, L_H + 8192); BAR();
    WAITV(0); BAR();
    BAR(); BAR(); BAR();
    bf16x8 qf[8];
    issueK(0, 1, 2);
#pragma unroll
    for (int ks = 0; ks < 8; ++ks) qf[ks] = lds128(L_H + w * 8192 + ks * 1024 + lane * 16);
    BAR();
    unsigned slot = 0;
    for (int p = 0; p < 4; ++p) {
      f32x16 o[4] = {};
      float m = -__builtin_inff(), l = 0.f;
      for (int tile = 0; tile < 8; ++tile) {
        unsigned s2 = slot == 0 ? 2 : slot - 1;
        if (p < 3) {
          if (tile == 0) issueX(p + 1, 1, L_X0);
          if (tile == 4) { issueX(p + 1, 2, L_H); issueX(p + 1, 3, L_H + 8192); issueX(p + 1, 4, L_H + 16384); issueX(p + 1, 5, L_H + 24576); }
          if (tile == 5) issueX(p + 1, 6, L_H);
        }
        const bool more = !(p == 3 && tile == 7);
        if (tile != 0 && more) issueK(p + (tile == 7), (tile + 1) & 7, s2);
        f32x16 p0 = {}, p1 = {};
        {
          const unsigned kb = laneK + L_KV + slot * 16384;
#pragma unroll
          for (int ks = 0; ks < 8; ++ks) {
            const unsigned a = kb ^ (ks << 5);
            bf16x8 a0 = lds128(a), a1 = lds128(a + 8192);
            p0 = MFMA32(a0, qf[ks], p0);
            p1 = MFMA32(a1, qf[ks], p1);
          }
        }
        float mx = p0[0];
#pragma unroll
        for (int i = 1; i < 16; ++i) mx = fmaxf(mx, p0[i]);
#pragma unroll
        for (int i = 0; i < 16; ++i) mx = fmaxf(mx, p1[i]);
        {
          auto rr = __builtin_amdgcn_permlane32_swap(__float_as_uint(mx), __float_as_uint(mx), false, false);
          mx = fmaxf(__uint_as_float(rr[0]), __uint_as_float(rr[1]));
        }
        const float mn = fmaxf(m, mx * SL2E);
        const float alpha = __builtin_amdgcn_exp2f(m - mn);
        m = mn;
        float ps = 0.f;
#pragma unroll
        for (int i = 0; i < 16; ++i) { p0[i] = __builtin_amdgcn_exp2f(fmaf(p0[i], SL2E, -mn)); ps += p0[i]; }
#pragma unroll
        for (int i = 0; i < 16; ++i) { p1[i] = __builtin_amdgcn_exp2f(fmaf(p1[i], SL2E, -mn)); ps += p1[i]; }
        l = l * alpha + ps;
        const bf16x8 pk0 = pack8<0>(p0), pk1 = pack8<8>(p0), pk2 = pack8<0>(p1), pk3 = pack8<8>(p1);
        if (more) { WAITV(4); } else { WAITV(0); }
        BAR();
        slot = slot == 2 ? 0 : slot + 1;
        s2 = slot == 0 ? 2 : slot - 1;
        if (p < 3) {
          if (tile == 4) issueX(p + 1, 8, L_X0);
          if (tile == 5) issueX(p + 1, 7, L_H + 8192);
        }
        if (more) issueV(p + (tile == 7), (tile + 1) & 7, s2);
        {
          const unsigned vb = laneV + L_KV + slot * 16384;
#pragma unroll
          for (int dt = 0; dt < 4; ++dt) {
            const unsigned a = vb ^ (dt << 6);
#pragma unroll
            for (int i = 0; i < 16; ++i) o[dt][i] *= alpha;
            o[dt] = MFMA32(tr2(a, a + 2048), pk0, o[dt]);
            o[dt] = MFMA32(tr2(a + 4096, a + 6144), pk1, o[dt]);
            o[dt] = MFMA32(tr2(a + 8192, a + 10240), pk2, o[dt]);
            o[dt] = MFMA32(tr2(a + 12288, a + 14336), pk3, o[dt]);
          }
        }
        if (more) { WAITV(4); } else { WAITV(0); }
        BAR();
        slot = slot == 2 ? 0 : slot + 1;
      }
      {
        const unsigned s2 = slot == 0 ? 2 : slot - 1;
        if (p < 3) issueK(p + 1, 1, s2);
        auto rr = __builtin_amdgcn_permlane32_swap(__float_as_uint(l), __float_as_uint(l), false, false);
        const float inv = 1.f / (__uint_as_float(rr[0]) + __uint_as_float(rr[1]));
        const unsigned hb = L_H + w * 8192 + lane * 16;
        if (p < 3) {
#pragma unroll
          for (int ks = 0; ks < 8; ++ks) qf[ks] = lds128(hb + ks * 1024);
        }
        WAITL0();
#pragma unroll
        for (int dt = 0; dt < 4; ++dt) {
#pragma unroll
          for (int i = 0; i < 16; ++i) o[dt][i] *= inv;
          st128(hb + dt * 2048, pack8<0>(o[dt]));
          st128(hb + dt * 2048 + 1024, pack8<8>(o[dt]));
        }
        WAITV(4);
        BAR();
      }
    }
  } else {
    const int j = wid - 4;
    const char* WQ = (br ? wqB : wqA) + g * 2048 + j * 128;
    const char* WO = (br ? woB : woA) + (size_t)g * 1024 * 1152;
    const unsigned ring = L_W + j * 18432;
    const unsigned voffQ = (lane >> 3) * 8192 + (((lane & 7) ^ ((g4 & 1) << 2)) * 16);
    const unsigned voffO = (lane >> 3) * 1152 + j * 256 + (((lane & 7) ^ ((g4 & 1) << 1)) * 16);
    const unsigned pos = lane >> 1;
    const unsigned voffC = (pos ^ (((pos >> 3) & 1) << 2)) * 1152 + 1024 + j * 32 + (lane & 1) * 16;
    const unsigned laneOA = ring + g4 * 1024 + q4 * 128 + ((((q4 >> 1) & 1) | ((g4 & 1) << 1)) * 32) + p4 * 8;
    const unsigned laneOC = ring + 8192 + (8 * g4 + 4 * (g4 & 1) + q4) * 32 + p4 * 8;
    const unsigned laneOB = (L_H + (g4 >> 1) * 1024 + (g4 & 1) * 512 + i16 * 16) ^ (((g >> 1) << 13) | ((g & 1) << 8));
    const unsigned laneQA = ring + h * 1024 + q4 * 128 + (((q4 >> 1) & 1) * 64) + gi * 32 + pp * 8;
    const unsigned laneQB = r * 128 + ((h ^ ((r >> 1) & 7)) * 16);
    f32x4 acc[9][4] = {};
    f32x16 aq[2][2] = {};
    const char* nbase; unsigned ngstep, npstep, ve, vo; bool nisO;
    auto src = [&](int n) {
      n = n < 67 ? n : 67;
      const int blk = n >= 35, mm = n - 18 - 17 * blk;
      const bool lo = n < 18, hi = n >= 52;
      nisO = !lo && (hi || (mm >= 1 && mm <= 8));
      const int uq = lo ? (n >= 9) : 2 + blk, sq = lo ? n - 9 * (n >= 9) : (mm == 0 ? 0 : mm - 8);
      const int uo = hi ? 2 + (n >= 60) : blk, so = hi ? ((n - 52) & 7) : mm - 1;
      const unsigned offq = (unsigned)xblk(r0, uq, sq) * (64u * 8192u) + ((uq + rot) & 3) * 512u;
      const unsigned offo = (unsigned)(((uo + rot) & 3) * 256 + so * 32) * 1152u;
      nbase = (nisO ? WO : WQ) + (nisO ? offo : offq);
      ngstep = nisO ? 128u : 32u * 8192u; npstep = nisO ? 9216u : 65536u;
      ve = nisO ? voffO : voffQ; vo = nisO ? (voffO ^ 64) : voffQ;
    };
    auto refill = [&](int G, unsigned half) {
      const unsigned l0 = ring + half + G * 4096;
      if (G < 2) {
        const char* p = nbase + G * ngstep;
        dma(l0, p, ve); dma(l0 + 1024, p + npstep, vo); dma(l0 + 2048, p + 2 * npstep, ve); dma(l0 + 3072, p + 3 * npstep, vo);
      } else if (nisO) {
        dma(l0, nbase, voffC);
      }
    };
    src(0); refill(0, 0); refill(1, 0); src(1); refill(0, 9216); refill(1, 9216);
    BAR();
    int n = 0;
#pragma nounroll
    for (int p = -2; p <= 4; ++p) {
      if (p >= 1) {
#pragma nounroll
        for (int s = 0; s < 8; ++s) {
          const unsigned half = (n & 1) ? 9216u : 0u;
          src(n + 2);
          unsigned ba = laneOB + s * 2048 + (s >> 2) * 8192;
          unsigned aa = laneOA + half, ac = laneOC + half;
          asm volatile("" : "+v"(ba), "+v"(aa), "+v"(ac));
          bf16x8 bq[4];
          bq[0] = lds128(ba); bq[1] = lds128(ba ^ 256); bq[2] = lds128(ba ^ 8192); bq[3] = lds128(ba ^ 8448);
#pragma unroll
          for (int G = 0; G < 2; ++G) {
            WAITV(12);
#pragma unroll
            for (int t = 0; t < 4; ++t) {
              const unsigned a = (aa ^ (t << 5)) + G * 4096;
              const bf16x8 af = tr2(a, a + 512);
#pragma unroll
              for (int qt = 0; qt < 4; ++qt) acc[G * 4 + t][qt] = MFMA16(af, bq[qt], acc[G * 4 + t][qt]);
              if (t == 1) SBAR();
            }
            WAITL0();
            refill(G, half);
          }
          WAITV(12);
          {
            const bf16x8 af = tr2(ac, ac ^ 128);
#pragma unroll
            for (int qt = 0; qt < 4; ++qt) acc[8][qt] = MFMA16(af, bq[qt], acc[8][qt]);
          }
          WAITL0();
          refill(2, half);
          ++n;
          if (p < 4) BAR();
        }
        if (p == 4) break;
      } else if (p == 0) {
        BAR(); BAR(); BAR(); BAR(); BAR(); BAR(); BAR(); BAR();
      }
#pragma nounroll
      for (int t = (p == -2 ? 16 : 8); t <= 16; ++t) {
        if (p <= (t < 16 ? 2 : 1)) {
          const unsigned half = (n & 1) ? 9216u : 0u;
          src(n + 2);
          unsigned xb = (t >= 9 && t <= 14) ? L_H + ((t - 9) & 3) * 8192 : L_X0;
          if (n == 1) xb = L_KV + 2 * 16384;
          unsigned aa = laneQA + half, bb = laneQB + xb;
          asm volatile("" : "+v"(aa), "+v"(bb));
#pragma unroll
          for (int G = 0; G < 2; ++G) {
            WAITV(12);
#pragma unroll
            for (int k2 = 0; k2 < 2; ++k2) {
              const int ks = G * 2 + k2;
              const unsigned a = aa + ks * 2048, bx = bb ^ (ks << 5);
              const bf16x8 a0 = tr2(a, a + 512), a1 = tr2((aa ^ 64) + ks * 2048, (aa ^ 64) + ks * 2048 + 512);
              const bf16x8 b0 = lds128(bx), b1 = lds128(bx + 4096);
              aq[0][0] = MFMA32(a0, b0, aq[0][0]); aq[0][1] = MFMA32(a0, b1, aq[0][1]);
              aq[1][0] = MFMA32(a1, b0, aq[1][0]); aq[1][1] = MFMA32(a1, b1, aq[1][1]);
              SBAR();
            }
            WAITL0();
            refill(G, half);
          }
          refill(2, half);
          if (t == 15) {
            unsigned ln = lane;
            asm volatile("" : "+v"(ln));
            const unsigned d = L_H + (j >> 1) * 16384 + (j & 1) * 4096 + ln * 16;
#pragma unroll
            for (int d2 = 0; d2 < 2; ++d2)
#pragma unroll
              for (int qt = 0; qt < 2; ++qt) {
                st128(d + qt * 8192 + d2 * 2048, pack8<0>(aq[d2][qt]));
                st128(d + qt * 8192 + d2 * 2048 + 1024, pack8<8>(aq[d2][qt]));
                aq[d2][qt] = f32x16{};
              }
          }
          ++n;
        }
        BAR();
      }
    }
    const __amdgpu_buffer_rsrc_t rs = __builtin_amdgcn_make_buffer_rsrc((void*)(ws + WS_SLAB), 0, (int)WS_SLAB_BYTES, 0x00020000);
#pragma unroll
    for (int i = 1; i < 4; ++i) {
      const int so = (((bid ^ i) * 3 + (i - 1)) * 4 + j) * 9216;
#pragma unroll
      for (int t = 0; t < 9; ++t) __builtin_amdgcn_raw_buffer_store_b128(__builtin_bit_cast(u32x4, acc[t][i]), rs, lane * 16 + t * 1024, so, 16);
    }
    WAITV(0);
    GAS unsigned* flags = (GAS unsigned*)(ws + WS_FLAG);
    if (lane >= 1 && lane < 4) __hip_atomic_store(flags + ((bid ^ lane) * 4 + j) * 16 + lane, 1u, __ATOMIC_RELAXED, __HIP_MEMORY_SCOPE_AGENT);
    {
      GAS unsigned* mine = flags + (bid * 4 + j) * 16 + (lane & 3);
      for (unsigned spins = 0; spins < (1u << 20); ++spins) {
        unsigned v = 1u;
        if (lane >= 1 && lane < 4) v = __hip_atomic_load(mine, __ATOMIC_RELAXED, __HIP_MEMORY_SCOPE_AGENT);
        if (__all(v == 1u)) break;
        __builtin_amdgcn_s_sleep(1);
      }
      __builtin_amdgcn_fence(__ATOMIC_ACQUIRE, "agent");
    }
#pragma unroll
    for (int i = 1; i < 4; ++i) {
      const int so = ((bid * 3 + (i - 1)) * 4 + j) * 9216;
#pragma unroll
      for (int t = 0; t < 9; ++t) acc[t][i] = __builtin_bit_cast(f32x4, __builtin_amdgcn_raw_buffer_load_b128(rs, lane * 16 + t * 1024, so, 16));
    }
    char* orow = out + ((size_t)(b * 64 + 16 * g + i16) * 1152 + br * 576 + 4 * g4) * 2;
#pragma unroll
    for (int t = 0; t < 9; ++t) {
      const f32x4 v = ((acc[t][0] + acc[t][1]) + acc[t][2]) + acc[t][3];
      const int n0 = t < 8 ? 128 * j + 16 * t : 512 + 16 * j;
      u32x2 pk = {cvtpk(v[0], v[1]), cvtpk(v[2], v[3])};
      *(GAS u32x2*)(orow + n0 * 2) = pk;
    }
  }
}

extern "C" void kernel_launch(void* const* d_in, const int* in_sizes, int n_in, void* d_out, int out_size, void* d_ws, size_t ws_size,
                              hipStream_t stream) {
  (void)in_sizes; (void)n_in; (void)out_size; (void)ws_size;
  (void)hipFuncSetAttribute((const void*)fused_gqa, hipFuncAttributeMaxDynamicSharedMemorySize, (int)L_TOT);
  (void)hipMemsetAsync((char*)d_ws + WS_FLAG, 0, WS_FLAG_BYTES, stream);
  hipLaunchKernelGGL(fused_gqa, dim3(256), dim3(512), L_TOT, stream, (const char*)d_in[0], (const char*)d_in[1], (const char*)d_in[2],
                     (const char*)d_in[3], (const char*)d_in[4], (const char*)d_in[5], (const char*)d_in[6], (const char*)d_in[7],
                     (const char*)d_in[8], (const char*)d_in[9], (char*)d_out, (char*)d_ws);
}
```

```cpp
#include <hip/hip_runtime.h>
#include <hip/hip_bf16.h>
#include <stdint.h>

typedef __attribute__((ext_vector_type(4))) short s16x4;
typedef __attribute__((ext_vector_type(8))) short bf16x8;
typedef __attribute__((ext_vector_type(4))) float f32x4;
typedef __attribute__((ext_vector_type(16))) float f32x16;
typedef __attribute__((ext_vector_type(4))) unsigned u32x4;
typedef __attribute__((ext_vector_type(2))) unsigned u32x2;
#define LAS __attribute__((address_space(3)))
#define GAS __attribute__((address_space(1)))
#define DEVI __device__ __forceinline__

constexpr unsigned L_H = 0;
constexpr unsigned RG = 10240;
constexpr unsigned L_KV = 40960;
constexpr unsigned L_W = 90112;
constexpr unsigned L_TOT = 163840;
constexpr size_t WS_SLAB = 0;
constexpr size_t WS_SLAB_BYTES = (size_t)256 * 3 * 4 * 4608;
constexpr size_t WS_FLAG = (size_t)32 << 20;
constexpr size_t WS_FLAG_BYTES = (size_t)256 * 4 * 64;

constexpr float SL2E = 0.088388347648318447f * 1.4426950408889634f;

DEVI bf16x8 lds128(unsigned a) { return *(LAS bf16x8*)(a); }
DEVI void st128(unsigned a, bf16x8 v) { *(LAS bf16x8*)(a) = v; }
DEVI bf16x8 tr2(unsigned a0, unsigned a1) {
  s16x4 lo = __builtin_amdgcn_ds_read_tr16_b64_v4i16((LAS s16x4*)(a0));
  s16x4 hi = __builtin_amdgcn_ds_read_tr16_b64_v4i16((LAS s16x4*)(a1));
  return (bf16x8){lo[0], lo[1], lo[2], lo[3], hi[0], hi[1], hi[2], hi[3]};
}
typedef __attribute__((ext_vector_type(2))) float f32x2;
typedef __attribute__((ext_vector_type(2))) __bf16 bf16x2;
DEVI unsigned cvtpk(float lo, float hi) { return __builtin_bit_cast(unsigned, __builtin_convertvector((f32x2){lo, hi}, bf16x2)); }
#define PIN(x) asm volatile("" : "+v"(x))
template <int B> DEVI bf16x8 pack8(const f32x16& v) {
  u32x4 w = {cvtpk(v[B], v[B + 1]), cvtpk(v[B + 2], v[B + 3]), cvtpk(v[B + 4], v[B + 5]), cvtpk(v[B + 6], v[B + 7])};
  return __builtin_bit_cast(bf16x8, w);
}
template <int OFF> DEVI void dma_nt2(const char* sb, unsigned voff) {
  asm volatile("global_load_lds_dwordx4 %0, %1 offset:%2 nt" ::"v"(voff), "s"(sb), "i"(OFF) : "memory");
}
template <int OFF> DEVI void dma_nt(unsigned lds, const char* sb, unsigned voff) {
  asm volatile("s_mov_b32 m0, %0\n\ts_nop 0\n\tglobal_load_lds_dwordx4 %1, %2 offset:%3 nt" ::"s"(lds), "v"(voff), "s"(sb), "i"(OFF) : "memory");
}
DEVI void dma(unsigned lds, const char* sb, unsigned voff) {
  asm volatile("s_mov_b32 m0, %0\n\ts_nop 0\n\tglobal_load_lds_dwordx4 %1, %2" ::"s"(lds), "v"(voff), "s"(sb) : "memory");
}
template <int OFF> DEVI void bdma(u32x4 rs, unsigned soff, unsigned voff) {
  asm volatile("buffer_load_dwordx4 %0, %1, %2 offen offset:%3 lds" ::"v"(voff), "s"(rs), "s"(soff), "i"(OFF) : "memory");
}
#define SETM0(x) asm volatile("s_mov_b32 m0, %0\n\ts_nop 0" ::"s"(x) : "memory")
#define PINS(x) asm volatile("" : "+s"(x))
#define WAITV(n) asm volatile("s_waitcnt vmcnt(" #n ")" ::: "memory")
#define WAITL0() asm volatile("s_waitcnt lgkmcnt(0)" ::: "memory")
#define BAR() asm volatile("s_waitcnt lgkmcnt(0)\n\ts_barrier" ::: "memory")
#define SBAR() __builtin_amdgcn_sched_barrier(0)
#define WL(n) __builtin_amdgcn_s_waitcnt(0xC07F | ((n) << 8))
#define MFMA32(a, b, c) __builtin_amdgcn_mfma_f32_32x32x16_bf16(a, b, c, 0, 0, 0)
#define MFMA16(a, b, c) __builtin_amdgcn_mfma_f32_16x16x32_bf16(a, b, c, 0, 0, 0)

DEVI int lane_id() { int l; asm volatile("v_mbcnt_lo_u32_b32 %0, -1, 0\n\tv_mbcnt_hi_u32_b32 %0, -1, %0" : "=v"(l)); return l; }
#define STAMP_DECL()
#define MT(i)
#define MBAR() BAR()
#define LBAR(T) BAR()
#define STAMP_OUT()
#define ASTAMP_DECL()
#define ASTAMP_T(i)
#define ABAR() BAR()
#define AWAITV(n) WAITV(n)
#define ASTAMP_OUT()
DEVI int xblk(int r0, int unit, int s) { int v = r0 + 9 - unit + s; v -= (v >= 18) ? 18 : 0; v -= (v >= 9) ? 9 : 0; return v; }

__global__ __launch_bounds__(512, 2) void fused_gqa(const char* __restrict__ xA, const char* __restrict__ kA, const char* __restrict__ vA,
                                                    const char* __restrict__ wqA, const char* __restrict__ woA, const char* __restrict__ xB,
                                                    const char* __restrict__ kB, const char* __restrict__ vB, const char* __restrict__ wqB,
                                                    const char* __restrict__ woB, char* __restrict__ out, char* __restrict__ ws) {
  extern __shared__ __attribute__((aligned(16))) char shm[];
  const int bid = blockIdx.x, br = (bid >> 2) & 1, g = bid & 3, b = bid >> 3;
  const int wid = __builtin_amdgcn_readfirstlane(threadIdx.x >> 6);
  const int rot = b & 3, r0 = b >> 2;
#define LANE_CONSTS() const int lane = lane_id(); \
  const int r = lane & 31, h = lane >> 5, i16 = lane & 15, q4 = i16 >> 2, p4 = i16 & 3, pp = ((p4 & 1) << 1) | (p4 >> 1), gi = (lane >> 4) & 1, g4 = lane >> 4; \
  (void)r; (void)h; (void)q4; (void)p4; (void)pp; (void)gi; (void)g4;

  if (wid < 4) {
    const int w = wid;
    LANE_CONSTS();
    STAMP_DECL();
    const char* X = (br ? xB : xA) + (size_t)(b * 64 + 16 * w) * 1152;
    const char* Kc = (br ? kB : kA) + (size_t)(b * 16 + 4 * g) * 131072 + w * 4096;
    const char* Vc = (br ? vB : vA) + (size_t)(b * 16 + 4 * g) * 131072 + w * 4096;
    const unsigned laneK = r * 256 + ((h ^ (r & 15)) * 16);
    const unsigned laneV = (4 * h + q4) * 256 + q4 * 64 + gi * 32 + pp * 8;
    const unsigned voffK = g4 * 256 + ((i16 ^ g4) * 16);
    const unsigned voffV = g4 * 256 + ((i16 ^ (g4 << 2)) * 16);
    const unsigned voffX = (lane >> 3) * 1152 + (((lane & 7) ^ g4) * 16);
    const unsigned hb = L_H + w * RG + lane * 16;
    auto kvoff = [&](int idx) -> unsigned { idx = idx < 31 ? idx : 31; return (unsigned)((idx + 8 * rot) & 31) * 16384u; };
    auto issueX = [&](int unit, int s, int slot) {
      const char* p = X + xblk(r0, unit, s) * 128;
      const unsigned d = L_H + w * RG + slot * 2048;
      dma(d, p, voffX); dma(d + 1024, p + 9216, voffX ^ 64);
    };
#define KPIECE(c, d, s) do { if ((c) == 0) dma_nt<0>(d, s, voffK); else dma_nt2<(c) * 1024>(s, voffK ^ ((c) * 64)); } while (0)
#define VPIECE(c, d, s) do { if ((c) == 0) dma_nt<0>(d, s, voffV); else dma_nt2<(c) * 1024>(s, voffV); } while (0)
#define RDK(kb, i) lds128(((kb) ^ (((i) >> 1) << 5)) + ((i) & 1) * 8192)
#define RDV(vb, i) tr2(((vb) ^ (((i) & 3) << 6)) + ((i) >> 2) * 4096, ((vb) ^ (((i) & 3) << 6)) + ((i) >> 2) * 4096 + 2048)
    bf16x8 qf[8], fr[8], pk0 = {}, pk1 = {}, pk2, pk3;
    f32x16 o[4] = {}, sa0, sa1, sb0 = {}, sb1 = {}, nmv = {};
    float l = 1.f, alpha = 1.f, mx = 0.f;
    bool resc = false;
#define EXP1(S, i) do { float t_ = S[i]; PIN(t_); t_ = __builtin_amdgcn_exp2f(t_); l += t_; S[i] = t_; } while (0)
#define EXPE(S0, S1, e) do { if ((e) < 16) EXP1(S0, (e) & 15); else EXP1(S1, (e) & 15); } while (0)
#define MAXG(S0, S1, gq) do { \
      if ((gq) == 1) { mx = fmaxf(fmaxf(S0[0], S0[1]), S0[2]); _Pragma("unroll") for (int i_ = 3; i_ < 13; i_ += 2) mx = fmaxf(fmaxf(mx, S0[i_]), S0[i_ + 1]); } \
      if ((gq) == 2) { mx = fmaxf(fmaxf(mx, S0[13]), S0[14]); mx = fmaxf(fmaxf(mx, S0[15]), S1[0]); _Pragma("unroll") for (int i_ = 1; i_ < 7; i_ += 2) mx = fmaxf(fmaxf(mx, S1[i_]), S1[i_ + 1]); } \
      if ((gq) == 3) { _Pragma("unroll") for (int i_ = 7; i_ < 15; i_ += 2) mx = fmaxf(fmaxf(mx, S1[i_]), S1[i_ + 1]); mx = fmaxf(mx, S1[15]); } \
      PIN(mx); } while (0)
#define REFUPD(S0, S1, NEWU) do { \
      if (NEWU || __builtin_amdgcn_ballot_w64(mx > 8.f) != 0) { \
        auto rr_ = __builtin_amdgcn_permlane32_swap(__float_as_uint(mx), __float_as_uint(mx), false, false); \
        float d_ = fmaxf(__uint_as_float(rr_[0]), __uint_as_float(rr_[1])); \
        if (NEWU) { \
          auto r2_ = __builtin_amdgcn_permlane32_swap(__float_as_uint(l), __float_as_uint(l), false, false); \
          alpha = __builtin_amdgcn_rcpf(__uint_as_float(r2_[0]) + __uint_as_float(r2_[1])); l = 0.f; \
        } else { d_ = fmaxf(d_, 0.f); alpha = __builtin_amdgcn_exp2f(-d_); l *= alpha; resc = true; } \
        const float n_ = (NEWU ? 0.f : nmv[0]) - d_; \
        _Pragma("unroll") for (int i_ = 0; i_ < 16; ++i_) { S0[i_] -= d_; S1[i_] -= d_; nmv[i_] = n_; } \
      } } while (0)
    issueX(0, 0, 4); issueX(0, 1, 0); issueX(0, 2, 1); issueX(0, 3, 2); issueX(0, 4, 3);
    {
      const unsigned d = L_KV + w * 4096;
      const char* s = Kc + kvoff(0);
      KPIECE(0, d, s); KPIECE(1, d, s); KPIECE(2, d, s); KPIECE(3, d, s);
      s = Vc + kvoff(0);
      VPIECE(0, d + 16384, s); VPIECE(1, d + 16384, s); VPIECE(2, d + 16384, s); VPIECE(3, d + 16384, s);
      s = Kc + kvoff(1);
      KPIECE(0, d + 32768, s); KPIECE(1, d + 32768, s); KPIECE(2, d + 32768, s); KPIECE(3, d + 32768, s);
    }
    MT(0); WAITV(20); MBAR(); MT(1);
    WAITV(18); MBAR();
    issueX(0, 8, 4); WAITV(18); MBAR();
    issueX(0, 5, 0); WAITV(18); MBAR();
    issueX(0, 6, 1); WAITV(18); MBAR();
    issueX(0, 7, 2); WAITV(4); MBAR();
    WAITV(2); MBAR();
    WAITV(0); MBAR();
    MBAR();
    MBAR();
    MT(2);
    {
#pragma unroll
      for (int ks = 0; ks < 8; ++ks) qf[ks] = lds128(hb + ks * 1024);
      WAITL0();
      issueX(1, 1, 0); issueX(1, 2, 1);
      const char* s = Kc + kvoff(1);
      const unsigned d = L_KV + w * 4096 + 32768;
      KPIECE(0, d, s); KPIECE(1, d, s); KPIECE(2, d, s); KPIECE(3, d, s);
      const unsigned kb = laneK + L_KV;
#pragma unroll
      for (int i = 0; i < 8; ++i) fr[i] = RDK(kb, i);
    }
    unsigned c0 = L_KV, c1 = L_KV + 16384, c2 = L_KV + 32768;
#define ROT() do { const unsigned t_ = c0; c0 = c1; c1 = c2; c2 = t_; } while (0)
#define TICK_A(D0, D1, E0, E1, T, CZ, FIN, XC, XS, XSLOT) do { \
      const unsigned kb_ = laneK + c0, vb_ = laneV + c1, dd_ = c0 + w * 4096; \
      const char* sp_ = Vc + kvoff((T) >> 1); \
      if (!(FIN) && resc) { \
        _Pragma("unroll") for (int e = 0; e < 64; ++e) o[e >> 4][e & 15] *= alpha; \
        resc = false; \
      } \
      SBAR(); \
      _Pragma("unroll") for (int i = 0; i < 16; ++i) { \
        if (i == 0 || i == 4 || i == 8) { WL(4); SBAR(); } \
        if (i & 1) D1 = MFMA32(fr[i & 7], qf[i >> 1], i < 2 ? (CZ ? f32x16{} : nmv) : D1); else D0 = MFMA32(fr[i & 7], qf[i >> 1], i < 2 ? (CZ ? f32x16{} : nmv) : D0); \
        if (i < 8) fr[i] = RDK(kb_, i + 8); \
        if (i == 10) { \
          SBAR(); WAITV(4); WL(0); LBAR(T); \
          fr[0] = RDV(vb_, 0); fr[1] = RDV(vb_, 1); fr[2] = RDV(vb_, 2); \
          if (XC) issueX(((T) >> 4) + 1, XS, XSLOT); \
        } \
        if (i > 10) fr[i - 8] = RDV(vb_, i - 8); \
        if (i == 11) VPIECE(0, dd_, sp_); if (i == 12) VPIECE(1, dd_, sp_); if (i == 13) VPIECE(2, dd_, sp_); if (i == 14) VPIECE(3, dd_, sp_); \
        if (i < 9) { EXPE(E0, E1, 14 + 2 * i); EXPE(E0, E1, 15 + 2 * i); PIN(l); } \
        if (i == 1) { pk1 = pack8<8>(E0); PIN(pk1); } \
        if (i == 5) { pk2 = pack8<0>(E1); PIN(pk2); } \
        if (i == 9) { pk3 = pack8<8>(E1); PIN(pk3); } \
        if (FIN && i >= 1 && i <= 8) { \
          _Pragma("unroll") for (int e = 0; e < 8; ++e) o[(i - 1) >> 1][((i - 1) & 1) * 8 + e] *= alpha; \
          st128(hb + (i - 1) * 1024, ((i - 1) & 1) ? pack8<8>(o[(i - 1) >> 1]) : pack8<0>(o[(i - 1) >> 1])); \
        } \
        SBAR(); \
      } \
      ROT(); } while (0)
#define TICK_B(S0, S1, T, newq, NEWU, OZ) do { \
      const unsigned vb_ = laneV + c0, kb_ = laneK + c1, dd_ = c0 + w * 4096; \
      const char* sp_ = Kc + kvoff(((T) + 3) >> 1); \
      _Pragma("unroll") for (int i = 0; i < 16; ++i) { \
        if (i == 0 || i == 4 || i == 8) { WL(8); SBAR(); } \
        o[i & 3] = MFMA32(fr[i & 7], (i < 4 ? pk0 : i < 8 ? pk1 : i < 12 ? pk2 : pk3), (OZ && i < 4) ? f32x16{} : o[i & 3]); \
        if (i < 8) fr[i] = RDV(vb_, i + 8); \
        if (i == 10) { \
          SBAR(); WAITV(4); WL(0); LBAR(T); \
          if (newq) { \
            _Pragma("unroll") for (int ks = 0; ks < 8; ++ks) qf[ks] = lds128(hb + ks * 1024); \
            WL(0); WAITL0(); \
            if ((T) < 47) { issueX((((T) + 1) >> 4) + 1, 1, 0); issueX((((T) + 1) >> 4) + 1, 2, 1); } \
          } \
          fr[0] = RDK(kb_, 0); fr[1] = RDK(kb_, 1); fr[2] = RDK(kb_, 2); \
        } \
        if (i > 10) fr[i - 8] = RDK(kb_, i - 8); \
        if (i == 11) KPIECE(0, dd_, sp_); if (i == 12) KPIECE(1, dd_, sp_); if (i == 13) KPIECE(2, dd_, sp_); if (i == 14) KPIECE(3, dd_, sp_); \
        if (i >= 1 && i <= 3) MAXG(S0, S1, i); \
        if (i == 4) { SBAR(); REFUPD(S0, S1, NEWU); } \
        if (i >= 5) { \
          constexpr int e0_[12] = {0, 1, 2, 3, 5, 7, 8, 9, 10, 11, 12, 14}; \
          _Pragma("unroll") for (int e = e0_[i - 5]; e < e0_[i - 4]; ++e) EXP1(S0, e); \
          PIN(l); } \
        if (i == 12) { pk0 = pack8<0>(S0); PIN(pk0); } \
        SBAR(); \
      } \
      ROT(); } while (0)
#pragma nounroll
    for (int P = 0; P < 5; ++P) {
      const int T0 = 16 * P;
      TICK_A(sa0, sa1, sb0, sb1, T0, true, false, P < 3, 3, 4);
      TICK_B(sa0, sa1, T0 + 1, false, true, false);
      TICK_A(sb0, sb1, sa0, sa1, T0 + 2, false, true, P < 3, 8, 4);
      if (P < 4) {
        TICK_B(sb0, sb1, T0 + 3, false, false, true);
#pragma nounroll
        for (int k = 0; k < 3; ++k) {
          const int T = T0 + 4 + 4 * k;
          const bool xc = P < 3 && k < 2;
          TICK_A(sa0, sa1, sb0, sb1, T, false, false, xc, 4 + 2 * k, 2 * k);
          TICK_B(sa0, sa1, T + 1, false, false, false);
          TICK_A(sb0, sb1, sa0, sa1, T + 2, false, false, xc, 5 + 2 * k, 2 * k + 1);
          TICK_B(sb0, sb1, T + 3, k == 2 && P < 3, false, false);
        }
      }
    }
    MT(3); WAITV(0);
    STAMP_OUT();
  } else {
    const int j = wid - 4;
    f32x4 acc[9][4] = {};
    ASTAMP_DECL();
    {
    LANE_CONSTS();
    const char* WQ = (br ? wqB : wqA) + g * 2048 + j * 128;
    const char* WO = (br ? woB : woA) + (size_t)g * 1024 * 1152;
    const unsigned ring = L_W + j * 18432;
    const unsigned voffQ = (lane >> 3) * 8192 + (((lane & 7) ^ ((g4 & 1) << 2)) * 16);
    const unsigned dvo = voffQ ^ ((lane >> 3) * 1152 + j * 256 + (((lane & 7) ^ ((g4 & 1) << 1)) * 16));
    const unsigned pos = lane >> 1;
    const unsigned voffC = (pos ^ (((pos >> 3) & 1) << 2)) * 1152 + 1024 + j * 32 + (lane & 1) * 16;
    const int dOB = (g >> 1) ? -(int)RG : (int)RG;
    f32x16 aq[2][2] = {};
    auto isO = [](int n) -> int { return (int)(n >= 52) | (int)((unsigned)(n - 22) < 8u) | (int)((unsigned)(n - 39) < 8u); };
    auto woff = [&](int n) -> unsigned {
      const int qi = n - (n >= 30 ? 8 : 0) - (n >= 47 ? 8 : 0), u = qi / 9, s = qi - 9 * u;
      const int oi = n >= 52 ? n - 36 : n >= 39 ? n - 31 : n - 22;
      return isO(n) ? (unsigned)(((((oi >> 3) + rot) & 3) * 256 + (oi & 1) * 128 + (oi & 6) * 16) * 9) | 65536u
                    : (unsigned)(xblk(r0, u, s) * 4096 + ((u + rot) & 3) * 4);
    };
    auto build = [&](int base) -> unsigned {
      const int n = base + lane_id(), m = n + 1;
      const int qi = m - (m >= 30 ? 8 : 0) - (m >= 47 ? 8 : 0), u = qi / 9, s = (qi - 9 * u) & 15;
      const int oi = m >= 52 ? m - 36 : m >= 39 ? m - 31 : m - 22;
      const unsigned boff = isO(m) ? (unsigned)(((oi & 7) >> 1) + (oi & 1) * 10) : (unsigned)((u == 0 ? 0x421032104ull : 0x432104104ull) >> (4 * s)) & 15u;
      const unsigned qw = n == 8 || n == 17 || n == 34 || n == 51;
      const unsigned nba = !(qw || n == 10 || n == 19 || n == 36 || n >= 60);
      const unsigned last = qw || n == 21 || n == 29 || n == 38 || n == 46 || n == 67;
      return woff(n + 2 < 67 ? n + 2 : 67) | boff << 17 | nba << 21 | (unsigned)(n == 12 || n == 59) << 22 | last << 23 | qw << 24 | (unsigned)(n == 51) << 25 | (unsigned)isO(m) << 26;
    };
    u32x4 rs = {0u, 0u, 0x7fffffffu, 0x00020000u};
    unsigned soff, gstep, pstep, ve, vo; unsigned long long cmask;
    auto src = [&](unsigned D) {
      const bool io = (D & 65536u) != 0;
      const unsigned long long bp = (unsigned long long)(io ? WO : WQ);
      rs[0] = (unsigned)bp; rs[1] = (unsigned)(bp >> 32);
      soff = (D & 65535u) << 7; gstep = io ? 128u : 32u * 8192u; pstep = io ? 9216u - 1024u : 65536u - 1024u;
      cmask = io ? ~0ull : 0ull;
      const unsigned msk = io ? ~0u : 0u;
      ve = voffQ ^ (dvo & msk); vo = ve ^ (64u & msk);
      PINS(rs); PINS(soff); PINS(gstep); PINS(pstep); PINS(cmask); PIN(ve); PIN(vo);
    };
#define RF(G, c, half) do { if ((c) == 0) SETM0(ring + (half) + (G) * 4096); bdma<(c) * 1024>(rs, soff + (G) * gstep + (c) * pstep, ((c) & 1) ? vo : ve); } while (0)
#define RFC(half) asm volatile("s_mov_b32 m0, %0\n\ts_mov_b64 exec, %1\n\tbuffer_load_dwordx4 %2, %3, %4 offen lds\n\ts_mov_b64 exec, -1" ::"s"(ring + (half) + 8192), "s"(cmask), "v"(voffC), "s"(rs), "s"(soff) : "memory")
    src(woff(0)); RF(0, 0, 0); RF(0, 1, 0); RF(0, 2, 0); RF(0, 3, 0); RF(1, 0, 0); RF(1, 1, 0); RF(1, 2, 0); RF(1, 3, 0);
    src(woff(1)); RF(0, 0, 9216); RF(0, 1, 9216); RF(0, 2, 9216); RF(0, 3, 9216); RF(1, 0, 9216); RF(1, 1, 9216); RF(1, 2, 9216); RF(1, 3, 9216);
    unsigned tab = build(0);
    ABAR();
    unsigned half = 0, D = 4u << 17;
    int ti = 0;
#define XBAR() asm volatile("s_bitcmp0_b32 %0, 22\n\ts_cbranch_scc1 1f\n\ts_barrier\n\ts_barrier\n\ts_barrier\n\ts_barrier\n\ts_barrier\n\ts_barrier\n\ts_barrier\n\ts_barrier\n1:\n\t" \
      "s_bitcmp0_b32 %0, 21\n\ts_cbranch_scc1 2f\n\ts_barrier\n2:" ::"s"(D) : "memory", "scc")
#pragma nounroll
    for (int run = 0; run < 6; ++run) {
      {
#define QA(ks, d) tr2((aa ^ ((d) << 6)) + (ks) * 2048, (aa ^ ((d) << 6)) + (ks) * 2048 + 512)
#define QB(ks, q) lds128((bb ^ ((ks) << 5)) + (q) * 2 * RG)
#define QM1(d, q, fa, fb) do { aq[d][q] = MFMA32(fa, fb, aq[d][q]); SBAR(); } while (0)
        LANE_CONSTS();
        const unsigned laneQB = L_H + (r >> 4) * RG + (r & 15) * 128 + ((h ^ ((r >> 1) & 7)) * 16);
        unsigned aa = ring + h * 1024 + q4 * 128 + (((q4 >> 1) & 1) * 64) + gi * 32 + pp * 8 + half, bb = laneQB + (((D >> 17) & 15u) << 11);
        bf16x8 r0_, r1, r2, x0, x1, y0, y1;
        WAITV(12);
        r0_ = QA(0, 0); x0 = QB(0, 0); x1 = QB(0, 1); r1 = QA(0, 1); r2 = QA(1, 0); y0 = QB(1, 0); y1 = QB(1, 1); SBAR();
#pragma nounroll
        do {
          D = __builtin_amdgcn_readlane(tab, ti); ++ti;
          QM1(0, 0, r0_, x0); QM1(0, 1, r0_, x1); r0_ = QA(1, 1); src(D); SBAR();
          QM1(1, 0, r1, x0); QM1(1, 1, r1, x1); WAITV(8); r1 = QA(2, 0); x0 = QB(2, 0); x1 = QB(2, 1); SBAR();
          QM1(0, 0, r2, y0); QM1(0, 1, r2, y1); r2 = QA(3, 0); SBAR();
          QM1(1, 0, r0_, y0); RF(0, 0, half); QM1(1, 1, r0_, y1); r0_ = QA(2, 1); y0 = QB(3, 0); y1 = QB(3, 1); RF(0, 1, half); SBAR();
          QM1(0, 0, r1, x0); RF(0, 2, half); QM1(0, 1, r1, x1); r1 = QA(3, 1); RF(0, 3, half); SBAR();
          aa += 9216u - 2 * half; bb = laneQB + (((D >> 17) & 15u) << 11); PIN(aa); PIN(bb);
          QM1(1, 0, r0_, x0); QM1(1, 1, r0_, x1);
          WL(0); WAITL0(); XBAR(); WAITV(8);
          r0_ = QA(0, 0); x0 = QB(0, 0); x1 = QB(0, 1); SBAR();
          QM1(1, 0, r1, y0); RF(1, 0, half); QM1(1, 1, r1, y1); r1 = QA(0, 1); RF(1, 1, half); SBAR();
          QM1(0, 0, r2, y0); RF(1, 2, half); QM1(0, 1, r2, y1); r2 = QA(1, 0); y0 = QB(1, 0); y1 = QB(1, 1); RF(1, 3, half); RFC(half); SBAR();
          half ^= 9216u;
        } while (!(D & (1u << 23)));
        if (D & (1u << 24)) {
          const unsigned d = L_H + (j >> 1) * 2 * RG + (j & 1) * 4096 + lane_id() * 16;
#pragma unroll
          for (int d2 = 1; d2 >= 0; --d2)
#pragma unroll
            for (int qt = 0; qt < 2; ++qt) {
              aq[d2][qt] *= SL2E;
              st128(d + qt * RG + d2 * 2048, pack8<0>(aq[d2][qt]));
              st128(d + qt * RG + d2 * 2048 + 1024, pack8<8>(aq[d2][qt]));
              aq[d2][qt] = f32x16{};
            }
          ABAR();
          if (D & (1u << 25)) { tab = build(52); ti = 0; ABAR(); ABAR(); ABAR(); }
        }
      }
      if (D & (1u << 26)) {
#define OA(t) tr2((aa ^ (((t) & 3) << 5)) + ((t) >> 2) * 4096, (aa ^ (((t) & 3) << 5)) + ((t) >> 2) * 4096 + 512)
#define OM1(t, f, q) acc[t][q] = MFMA16(f, bq[q], acc[t][q])
#define OM2(t, f, q) do { OM1(t, f, q); OM1(t, f, q + 1); SBAR(); } while (0)
#define OM(t, f) do { OM2(t, f, 0); OM2(t, f, 2); } while (0)
        LANE_CONSTS();
        const unsigned laneOC = ring + 8192 + (8 * g4 + 4 * (g4 & 1) + q4) * 32 + p4 * 8;
        const unsigned laneOB = L_H + (g >> 1) * RG + (g4 >> 1) * 1024 + (g4 & 1) * 512 + (g & 1) * 256 + i16 * 16;
        unsigned aa = ring + g4 * 1024 + q4 * 128 + ((((q4 >> 1) & 1) | ((g4 & 1) << 1)) * 32) + p4 * 8 + half;
        unsigned ba = laneOB + (((D >> 17) & 15u) << 11), bb = ba + dOB;
        bf16x8 bq[4], f0, f1, f2;
        WAITV(12);
        f0 = OA(0); bq[0] = lds128(ba); bq[2] = lds128(bb); bq[1] = lds128(ba ^ 256); f1 = OA(1); bq[3] = lds128(bb ^ 256); f2 = OA(2); SBAR();
#pragma nounroll
        do {
          D = __builtin_amdgcn_readlane(tab, ti); ++ti;
          OM1(0, f0, 0); OM1(0, f0, 2); OM1(0, f0, 1); SBAR(); src(D); SBAR();
          OM1(1, f1, 0); OM1(1, f1, 2); OM1(1, f1, 1); OM1(0, f0, 3); SBAR(); f0 = OA(3); SBAR();
          OM1(1, f1, 3); SBAR(); WAITV(8); f1 = OA(4); SBAR();
          OM(2, f2); f2 = OA(5); SBAR();
          OM2(3, f0, 0); RF(0, 0, half); OM2(3, f0, 2); f0 = OA(6); RF(0, 1, half); SBAR();
          OM2(4, f1, 0); RF(0, 2, half); OM2(4, f1, 2); f1 = OA(7); RF(0, 3, half); SBAR();
          OM(5, f2); WAITV(12); { const unsigned ac = laneOC + half; f2 = tr2(ac, ac ^ 128); } SBAR();
          aa += 9216u - 2 * half; ba = laneOB + (((D >> 17) & 15u) << 11); bb = ba + dOB; PIN(aa); PIN(ba); PIN(bb);
          OM(6, f0);
          OM1(7, f1, 0); OM1(7, f1, 2); OM1(8, f2, 0); OM1(8, f2, 2); SBAR();
          WL(0); WAITL0(); XBAR(); WAITV(8);
          f0 = OA(0); bq[0] = lds128(ba); bq[2] = lds128(bb); SBAR();
          OM1(7, f1, 1); SBAR(); RF(1, 0, half); OM1(8, f2, 1); SBAR(); bq[1] = lds128(ba ^ 256); RF(1, 1, half); SBAR();
          OM1(7, f1, 3); SBAR(); f1 = OA(1); RF(1, 2, half); OM1(8, f2, 3); SBAR(); bq[3] = lds128(bb ^ 256); f2 = OA(2); RF(1, 3, half); RFC(half); SBAR();
          half ^= 9216u;
        } while (!(D & (1u << 23)));
      }
    }
    }
    LANE_CONSTS();
    ASTAMP_T(3);
    WAITV(0);
    const __amdgpu_buffer_rsrc_t rs = __builtin_amdgcn_make_buffer_rsrc((void*)(ws + WS_SLAB), 0, (int)WS_SLAB_BYTES, 0x00020000);
#pragma unroll
    for (int i = 1; i < 4; ++i) {
      const int so = (((bid ^ i) * 3 + (i - 1)) * 4 + j) * 4608;
#pragma unroll
      for (int t = 0; t < 8; t += 2) {
        const u32x4 v = {cvtpk(acc[t][i][0], acc[t][i][1]), cvtpk(acc[t][i][2], acc[t][i][3]), cvtpk(acc[t + 1][i][0], acc[t + 1][i][1]), cvtpk(acc[t + 1][i][2], acc[t + 1][i][3])};
        __builtin_amdgcn_raw_buffer_store_b128(v, rs, lane * 16 + t * 512, so, 16);
      }
      const u32x2 v = {cvtpk(acc[8][i][0], acc[8][i][1]), cvtpk(acc[8][i][2], acc[8][i][3])};
      __builtin_amdgcn_raw_buffer_store_b64(v, rs, lane * 8 + 4096, so, 16);
    }
    WAITV(0);
    ASTAMP_T(4);
    GAS unsigned* flags = (GAS unsigned*)(ws + WS_FLAG);
    if (lane >= 1 && lane < 4) __hip_atomic_store(flags + ((bid ^ lane) * 4 + j) * 16 + lane, 1u, __ATOMIC_RELAXED, __HIP_MEMORY_SCOPE_AGENT);
    {
      GAS unsigned* mine = flags + (bid * 4 + j) * 16 + (lane & 3);
      for (unsigned spins = 0; spins < (1u << 20); ++spins) {
        unsigned v = 1u;
        if (lane >= 1 && lane < 4) v = __hip_atomic_load(mine, __ATOMIC_RELAXED, __HIP_MEMORY_SCOPE_AGENT);
        if (__all(v == 1u)) break;
        __builtin_amdgcn_s_sleep(1);
      }
      __builtin_amdgcn_fence(__ATOMIC_ACQUIRE, "agent");
    }
    ASTAMP_T(5);
    u32x4 pv[3][4]; u32x2 pw[3];
#pragma unroll
    for (int i = 1; i < 4; ++i) {
      const int so = ((bid * 3 + (i - 1)) * 4 + j) * 4608;
#pragma unroll
      for (int t = 0; t < 4; ++t) pv[i - 1][t] = __builtin_amdgcn_raw_buffer_load_b128(rs, lane * 16 + t * 1024, so, 16);
      pw[i - 1] = __builtin_amdgcn_raw_buffer_load_b64(rs, lane * 8 + 4096, so, 16);
    }
    char* orow = out + ((size_t)(b * 64 + 16 * g + i16) * 1152 + br * 576 + 4 * g4) * 2;
#pragma unroll
    for (int t = 0; t < 9; ++t) {
      f32x4 v = acc[t][0];
#pragma unroll
      for (int i = 0; i < 3; ++i) {
        const unsigned lo = t < 8 ? pv[i][t >> 1][(t & 1) * 2] : pw[i][0], hi = t < 8 ? pv[i][t >> 1][(t & 1) * 2 + 1] : pw[i][1];
        v[0] += __uint_as_float(lo << 16); v[1] += __uint_as_float(lo & 0xffff0000u);
        v[2] += __uint_as_float(hi << 16); v[3] += __uint_as_float(hi & 0xffff0000u);
      }
      const int n0 = t < 8 ? 128 * j + 16 * t : 512 + 16 * j;
      u32x2 pk = {cvtpk(v[0], v[1]), cvtpk(v[2], v[3])};
      *(GAS u32x2*)(orow + n0 * 2) = pk;
    }
    ASTAMP_OUT();
  }
}

extern "C" void kernel_launch(void* const* d_in, const int* in_sizes, int n_in, void* d_out, int out_size, void* d_ws, size_t ws_size,
                              hipStream_t stream) {
  (void)in_sizes; (void)n_in; (void)out_size; (void)ws_size;
  (void)hipFuncSetAttribute((const void*)fused_gqa, hipFuncAttributeMaxDynamicSharedMemorySize, (int)L_TOT);
  (void)hipMemsetAsync((char*)d_ws + WS_FLAG, 0, WS_FLAG_BYTES, stream);
  hipLaunchKernelGGL(fused_gqa, dim3(256), dim3(512), L_TOT, stream, (const char*)d_in[0], (const char*)d_in[1], (const char*)d_in[2],
                     (const char*)d_in[3], (const char*)d_in[4], (const char*)d_in[5], (const char*)d_in[6], (const char*)d_in[7],
                     (const char*)d_in[8], (const char*)d_in[9], (char*)d_out, (char*)d_ws);
}
```

```cpp
#include <hip/hip_runtime.h>
#include <stdint.h>
#ifdef __HIP_DEVICE_COMPILE__
#pragma clang attribute push(__attribute__((target("no-packed-fp32-ops"))), apply_to = function)
#endif
#define U2F(x) __builtin_bit_cast(float, (unsigned)(x))
#define F2U(x) __builtin_bit_cast(unsigned, (float)(x))
#define ALL(c) (__builtin_amdgcn_ballot_w64(!(c)) == 0)

typedef __attribute__((ext_vector_type(4))) short s16x4;
typedef __attribute__((ext_vector_type(8))) short bf16x8;
typedef __attribute__((ext_vector_type(4))) float f32x4;
typedef __attribute__((ext_vector_type(16))) float f32x16;
typedef __attribute__((ext_vector_type(4))) unsigned u32x4;
typedef __attribute__((ext_vector_type(2))) unsigned u32x2;
#define LAS __attribute__((address_space(3)))
#define GAS __attribute__((address_space(1)))
#define DEVI __device__ __forceinline__

constexpr unsigned L_H = 0;
constexpr unsigned RG = 28672;
constexpr unsigned L_W = 10240;
constexpr unsigned L_KV = 114688;
constexpr unsigned L_TOT = 163840;
constexpr size_t WS_SLAB = 0;
constexpr size_t WS_SLAB_BYTES = (size_t)256 * 3 * 4 * 4608;
constexpr size_t WS_FLAG = (size_t)32 << 20;

constexpr float SL2E = 0.088388347648318447f * 1.4426950408889634f;

DEVI bf16x8 lds128(unsigned a) { return *(LAS bf16x8*)(a); }
DEVI void st128(unsigned a, bf16x8 v) { *(LAS bf16x8*)(a) = v; }
DEVI bf16x8 tr2(unsigned a0, unsigned a1) {
  s16x4 lo = __builtin_amdgcn_ds_read_tr16_b64_v4i16((LAS s16x4*)(a0));
  s16x4 hi = __builtin_amdgcn_ds_read_tr16_b64_v4i16((LAS s16x4*)(a1));
  return (bf16x8){lo[0], lo[1], lo[2], lo[3], hi[0], hi[1], hi[2], hi[3]};
}
typedef __attribute__((ext_vector_type(2))) float f32x2;
typedef __attribute__((ext_vector_type(2))) __bf16 bf16x2;
DEVI unsigned cvtpk(float lo, float hi) { return __builtin_bit_cast(unsigned, __builtin_convertvector((f32x2){lo, hi}, bf16x2)); }
#define PIN(x) asm volatile("" : "+v"(x))
template <int B> DEVI bf16x8 pack8(const f32x16& v) {
  u32x4 w = {cvtpk(v[B], v[B + 1]), cvtpk(v[B + 2], v[B + 3]), cvtpk(v[B + 4], v[B + 5]), cvtpk(v[B + 6], v[B + 7])};
  return __builtin_bit_cast(bf16x8, w);
}
template <int OFF> DEVI void dma_nt2(const char* sb, unsigned voff) {
  asm volatile("global_load_lds_dwordx4 %0, %1 offset:%2 nt" ::"v"(voff), "s"(sb), "i"(OFF) : "memory");
}
template <int OFF> DEVI void dma_nt(unsigned lds, const char* sb, unsigned voff) {
  asm volatile("s_mov_b32 m0, %0\n\ts_nop 0\n\tglobal_load_lds_dwordx4 %1, %2 offset:%3 nt" ::"s"(lds), "v"(voff), "s"(sb), "i"(OFF) : "memory");
}
DEVI void dma(unsigned lds, const char* sb, unsigned voff) {
  asm volatile("s_mov_b32 m0, %0\n\ts_nop 0\n\tglobal_load_lds_dwordx4 %1, %2" ::"s"(lds), "v"(voff), "s"(sb) : "memory");
}
template <int OFF> DEVI void bdma(u32x4 rs, unsigned soff, unsigned voff) {
  asm volatile("buffer_load_dwordx4 %0, %1, %2 offen offset:%3 lds" ::"v"(voff), "s"(rs), "s"(soff), "i"(OFF) : "memory");
}
#define SETM0(x) asm volatile("s_mov_b32 m0, %0\n\ts_nop 0" ::"s"(x) : "memory")
#define PINS(x) asm volatile("" : "+s"(x))
#define WAITV(n) asm volatile("s_waitcnt vmcnt(" #n ")" ::: "memory")
#define WAITL0() asm volatile("s_waitcnt lgkmcnt(0)" ::: "memory")
#define BAR() asm volatile("s_waitcnt lgkmcnt(0)\n\ts_barrier" ::: "memory")
#define SBAR() __builtin_amdgcn_sched_barrier(0)
#define WL(n) __builtin_amdgcn_s_waitcnt(0xC07F | ((n) << 8))
#define MFMA32(a, b, c) __builtin_amdgcn_mfma_f32_32x32x16_bf16(a, b, c, 0, 0, 0)
#define MFMA16(a, b, c) __builtin_amdgcn_mfma_f32_16x16x32_bf16(a, b, c, 0, 0, 0)

DEVI int lane_id() { int l; asm volatile("v_mbcnt_lo_u32_b32 %0, -1, 0\n\tv_mbcnt_hi_u32_b32 %0, -1, %0" : "=v"(l)); return l; }
#define STAMP_DECL()
#define MT(i)
#define MBAR() BAR()
#define LBAR(T) BAR()
#define STAMP_OUT()
#define ASTAMP_DECL()
#define ASTAMP_T(i)
#define ABAR() BAR()
#define AWAITV(n) WAITV(n)
#define ASTAMP_OUT()
DEVI int xblk(int r0, int unit, int s) { int v = r0 + 9 - unit + s; v -= (v >= 18) ? 18 : 0; v -= (v >= 9) ? 9 : 0; return v; }

__global__ __launch_bounds__(512, 2) void fused_gqa(const char* __restrict__ xA, const char* __restrict__ kA, const char* __restrict__ vA,
                                                    const char* __restrict__ wqA, const char* __restrict__ woA, const char* __restrict__ xB,
                                                    const char* __restrict__ kB, const char* __restrict__ vB, const char* __restrict__ wqB,
                                                    const char* __restrict__ woB, char* __restrict__ out, char* __restrict__ ws) {
  extern __shared__ __attribute__((aligned(16))) char shm[];
  const int bid = (int)__builtin_amdgcn_workgroup_id_x(), br = (bid >> 2) & 1, g = bid & 3, b = bid >> 3;
  const int wid = __builtin_amdgcn_readfirstlane((int)__builtin_amdgcn_workitem_id_x() >> 6);
  const int rot = b & 3, r0 = b >> 2;
#define LANE_CONSTS() const int lane = lane_id(); \
  const int r = lane & 31, h = lane >> 5, i16 = lane & 15, q4 = i16 >> 2, p4 = i16 & 3, pp = ((p4 & 1) << 1) | (p4 >> 1), gi = (lane >> 4) & 1, g4 = lane >> 4; \
  (void)r; (void)h; (void)q4; (void)p4; (void)pp; (void)gi; (void)g4;

  if (wid < 4) {
    const int w = wid;
    LANE_CONSTS();
    STAMP_DECL();
    const char* X = (br ? xB : xA) + (size_t)(b * 64 + 16 * w) * 1152;
    const char* Kc = (br ? kB : kA) + (size_t)(b * 16 + 4 * g) * 131072 + w * 4096;
    const char* Vc = (br ? vB : vA) + (size_t)(b * 16 + 4 * g) * 131072 + w * 4096;
    const unsigned laneK = r * 256 + ((h ^ (r & 15)) * 16);
    const unsigned laneV = (4 * h + q4) * 256 + q4 * 64 + gi * 32 + pp * 8;
    const unsigned voffK = g4 * 256 + ((i16 ^ g4) * 16);
    const unsigned voffV = g4 * 256 + ((i16 ^ (g4 << 2)) * 16);
    const unsigned voffX = (lane >> 3) * 1152 + (((lane & 7) ^ g4) * 16);
    const unsigned hb = L_H + w * RG + lane * 16;
    auto kvoff = [&](int idx) -> unsigned { idx = idx < 31 ? idx : 31; return (unsigned)((idx + 8 * rot) & 31) * 16384u; };
    auto issueX = [&](int unit, int s, int slot) {
      const char* p = X + xblk(r0, unit, s) * 128;
      const unsigned d = L_H + w * RG + slot * 2048;
      dma(d, p, voffX); dma(d + (slot == 9 ? 9216 : 1024), p + 9216, voffX ^ 64);
    };
#define KPIECE(c, d, s) do { if ((c) == 0) dma_nt<0>(d, s, voffK); else dma_nt2<(c) * 1024>(s, voffK ^ ((c) * 64)); } while (0)
#define VPIECE(c, d, s) do { if ((c) == 0) dma_nt<0>(d, s, voffV); else dma_nt2<(c) * 1024>(s, voffV); } while (0)
#define RDK(kb, i) lds128(((kb) ^ (((i) >> 1) << 5)) + ((i) & 1) * 8192)
#define RDV(vb, i) tr2(((vb) ^ (((i) & 3) << 6)) + ((i) >> 2) * 4096, ((vb) ^ (((i) & 3) << 6)) + ((i) >> 2) * 4096 + 2048)
    bf16x8 qf[8], fr[8], pk0 = {}, pk1 = {}, pk2, pk3;
    f32x16 o[4] = {}, sa0, sa1, sb0 = {}, sb1 = {}, nmv = {};
    float l = 1.f, alpha = 1.f, mx = 0.f;
    bool resc = false;
#define EXP1(S, i) do { float t_ = S[i]; PIN(t_); t_ = __builtin_amdgcn_exp2f(t_); l += t_; S[i] = t_; } while (0)
#define EXPE(S0, S1, e) do { if ((e) < 16) EXP1(S0, (e) & 15); else EXP1(S1, (e) & 15); } while (0)
#define MAXG(S0, S1, gq) do { \
      if ((gq) == 1) { mx = __builtin_fmaxf(__builtin_fmaxf(S0[0], S0[1]), S0[2]); _Pragma("unroll") for (int i_ = 3; i_ < 13; i_ += 2) mx = __builtin_fmaxf(__builtin_fmaxf(mx, S0[i_]), S0[i_ + 1]); } \
      if ((gq) == 2) { mx = __builtin_fmaxf(__builtin_fmaxf(mx, S0[13]), S0[14]); mx = __builtin_fmaxf(__builtin_fmaxf(mx, S0[15]), S1[0]); _Pragma("unroll") for (int i_ = 1; i_ < 7; i_ += 2) mx = __builtin_fmaxf(__builtin_fmaxf(mx, S1[i_]), S1[i_ + 1]); } \
      if ((gq) == 3) { _Pragma("unroll") for (int i_ = 7; i_ < 15; i_ += 2) mx = __builtin_fmaxf(__builtin_fmaxf(mx, S1[i_]), S1[i_ + 1]); mx = __builtin_fmaxf(mx, S1[15]); } \
      PIN(mx); } while (0)
#define REFUPD(S0, S1, NEWU) do { \
      if (NEWU || __builtin_amdgcn_ballot_w64(mx > 8.f) != 0) { \
        auto rr_ = __builtin_amdgcn_permlane32_swap(F2U(mx), F2U(mx), false, false); \
        float d_ = __builtin_fmaxf(U2F(rr_[0]), U2F(rr_[1])); \
        if (NEWU) { \
          auto r2_ = __builtin_amdgcn_permlane32_swap(F2U(l), F2U(l), false, false); \
          alpha = __builtin_amdgcn_rcpf(U2F(r2_[0]) + U2F(r2_[1])); l = 0.f; \
        } else { d_ = __builtin_fmaxf(d_, 0.f); alpha = __builtin_amdgcn_exp2f(-d_); l *= alpha; resc = true; } \
        const float n_ = (NEWU ? 0.f : nmv[0]) - d_; \
        _Pragma("unroll") for (int i_ = 0; i_ < 16; ++i_) { S0[i_] -= d_; S1[i_] -= d_; nmv[i_] = n_; } \
      } } while (0)
    issueX(0, 0, 4); issueX(0, 1, 0);
    MT(0);
#pragma unroll
    for (int i = 0; i < 4; ++i) st128(L_KV + 16384 + w * 4096 + i * 1024 + lane * 16, bf16x8{});
    WAITV(2); MBAR(); MT(1);
    issueX(0, 2, 1); issueX(0, 3, 2); issueX(0, 4, 3);
    WAITV(6); MBAR();
    issueX(0, 8, 4); WAITV(6); MBAR();
    issueX(0, 5, 0); WAITV(6); MBAR();
    issueX(0, 6, 1); WAITV(6); MBAR();
    issueX(0, 7, 2);
    {
      const unsigned d = L_KV + w * 4096;
      const char* s = Kc + kvoff(0);
      KPIECE(0, d, s); KPIECE(1, d, s); KPIECE(2, d, s); KPIECE(3, d, s);
    }
    issueX(1, 1, 9);
    {
      const char* wo_ = (br ? woB : woA) + (size_t)g * 1024 * 1152 + b * 36864 + w * 9216;
      asm volatile("s_mov_b32 m0, %0\n\ts_nop 0\n\tglobal_load_lds_dword %1, %3\n\tglobal_load_lds_dword %2, %3"
                   ::"s"(L_KV + 32768 + w * 4096), "v"(lane * 128), "v"((lane & 7) * 128 + 8192), "s"(wo_) : "memory");
    }
    WAITV(12); MBAR();
    WAITV(10); MBAR();
    WAITV(8); MBAR();
    MBAR();
    WAITV(4);
    MBAR();
    MT(2);
    {
#pragma unroll
      for (int ks = 0; ks < 8; ++ks) qf[ks] = lds128(hb + ks * 1024);
      const char* s = Kc + kvoff(1);
      const unsigned d = L_KV + w * 4096 + 32768;
      KPIECE(0, d, s); KPIECE(1, d, s); KPIECE(2, d, s); KPIECE(3, d, s);
      const unsigned kb = laneK + L_KV;
#pragma unroll
      for (int i = 0; i < 8; ++i) fr[i] = RDK(kb, i);
    }
    unsigned c0 = L_KV, c1 = L_KV + 16384, c2 = L_KV + 32768;
#define ROT() do { const unsigned t_ = c0; c0 = c1; c1 = c2; c2 = t_; } while (0)
#define TICK_A(D0, D1, E0, E1, T, CZ, FIN, XC, XU, XS, XSLOT) do { \
      const unsigned kb_ = laneK + c0, vb_ = laneV + c1, dd_ = c0 + w * 4096; \
      const char* sp_ = Vc + kvoff((T) >> 1); \
      if (!(FIN) && resc) { \
        _Pragma("unroll") for (int e = 0; e < 64; ++e) o[e >> 4][e & 15] *= alpha; \
        resc = false; \
      } \
      SBAR(); \
      _Pragma("unroll") for (int i = 0; i < 16; ++i) { \
        if (i == 0 || i == 4 || i == 8) { if (FIN && i == 4) WL(5); else if (FIN && i == 8) WL(6); else WL(4); SBAR(); } \
        if (i & 1) D1 = MFMA32(fr[i & 7], qf[i >> 1], i < 2 ? (CZ ? f32x16{} : nmv) : D1); else D0 = MFMA32(fr[i & 7], qf[i >> 1], i < 2 ? (CZ ? f32x16{} : nmv) : D0); \
        if (i < 8) fr[i] = RDK(kb_, i + 8); \
        if (i == 10) { \
          SBAR(); if (CZ) { WAITV(8); if (!(XC)) WAITV(4); } else WAITV(4); WL(0); LBAR(T); \
          fr[0] = RDV(vb_, 0); fr[1] = RDV(vb_, 1); fr[2] = RDV(vb_, 2); \
        } \
        if (i > 10) fr[i - 8] = RDV(vb_, i - 8); \
        if (i == 11) VPIECE(0, dd_, sp_); if (i == 12) VPIECE(1, dd_, sp_); if (i == 13) VPIECE(2, dd_, sp_); if (i == 14) VPIECE(3, dd_, sp_); \
        if (i < 9) { EXPE(E0, E1, 14 + 2 * i); EXPE(E0, E1, 15 + 2 * i); PIN(l); } \
        if (i == 1) { pk1 = pack8<8>(E0); PIN(pk1); } \
        if (i == 5) { pk2 = pack8<0>(E1); PIN(pk2); } \
        if (i == 9) { pk3 = pack8<8>(E1); PIN(pk3); } \
        if (i == 15 && (XC)) issueX(XU, XS, XSLOT); \
        if (CZ && (i == 5 || i == 7) && (XC)) issueX(XU, i == 5 ? 2 : 3, i == 5 ? 0 : 1); \
        if (FIN && ((i >= 2 && i <= 8 && !(i & 1)) || (i >= 11 && i <= 14))) { \
          const int c_ = i <= 8 ? 3 + (i >> 1) : i - 11; \
          _Pragma("unroll") for (int e = 0; e < 8; ++e) o[c_ >> 1][(c_ & 1) * 8 + e] *= alpha; \
          st128(hb + c_ * 1024, (c_ & 1) ? pack8<8>(o[c_ >> 1]) : pack8<0>(o[c_ >> 1])); \
        } \
        SBAR(); \
      } \
      ROT(); } while (0)
#define TICK_B(S0, S1, T, newq, NEWU, OZ, XC) do { \
      const unsigned vb_ = laneV + c0, kb_ = laneK + c1, dd_ = c0 + w * 4096; \
      const char* sp_ = Kc + kvoff(((T) + 3) >> 1); \
      _Pragma("unroll") for (int i = 0; i < 16; ++i) { \
        if (i == 0 || i == 4 || i == 8) { if (OZ && i == 0) WL(12); else WL(8); SBAR(); } \
        o[i & 3] = MFMA32(fr[i & 7], (i < 4 ? pk0 : i < 8 ? pk1 : i < 12 ? pk2 : pk3), (OZ && i < 4) ? f32x16{} : o[i & 3]); \
        if (i < 8) fr[i] = RDV(vb_, i + 8); \
        if (i == 10) { \
          SBAR(); WAITV(6); if (!(XC)) WAITV(4); WL(0); LBAR(T); \
          fr[0] = RDK(kb_, 0); fr[1] = RDK(kb_, 1); fr[2] = RDK(kb_, 2); \
        } \
        if (i > 10) fr[i - 8] = RDK(kb_, i - 8); \
        if (i == 11) KPIECE(0, dd_, sp_); if (i == 12) KPIECE(1, dd_, sp_); if (i == 13) KPIECE(2, dd_, sp_); if (i == 14) KPIECE(3, dd_, sp_); \
        if (i >= 1 && i <= 3) MAXG(S0, S1, i); \
        if (i == 4) { SBAR(); REFUPD(S0, S1, NEWU); } \
        if (i >= 5) { \
          constexpr int e0_[12] = {0, 1, 2, 3, 5, 7, 8, 9, 10, 11, 12, 14}; \
          _Pragma("unroll") for (int e = e0_[i - 5]; e < e0_[i - 4]; ++e) EXP1(S0, e); \
          PIN(l); } \
        if (i == 12) { pk0 = pack8<0>(S0); PIN(pk0); } \
        SBAR(); \
      } \
      if (newq) { \
        asm volatile("s_barrier" ::: "memory"); \
        _Pragma("unroll") for (int ks = 0; ks < 8; ++ks) qf[ks] = lds128(hb + ks * 1024); \
      } \
      ROT(); } while (0)
#pragma nounroll
    for (int P = 0; P < 5; ++P) {
      const int T0 = 16 * P;
      TICK_A(sa0, sa1, sb0, sb1, T0, true, false, P < 3, P + 1, 8, 4);
      TICK_B(sa0, sa1, T0 + 1, false, true, false, P < 3);
      TICK_A(sb0, sb1, sa0, sa1, T0 + 2, false, true, false, 0, 0, 0);
      if (P < 4) {
        TICK_B(sb0, sb1, T0 + 3, false, false, true, false);
#pragma nounroll
        for (int k = 0; k < 3; ++k) {
          const int T = T0 + 4 + 4 * k;
          const int xu = P + (k == 2 ? 2 : 1);
          TICK_A(sa0, sa1, sb0, sb1, T, false, false, xu < 4, xu, k == 2 ? 1 : 4 + 2 * k, k == 2 ? 9 : 2 - 2 * k);
          TICK_B(sa0, sa1, T + 1, false, false, false, xu < 4);
          TICK_A(sb0, sb1, sa0, sa1, T + 2, false, false, P < 3 && k < 2, xu, 5 + 2 * k, 3 - 2 * k);
          TICK_B(sb0, sb1, T + 3, k == 2 && P < 3, false, false, P < 3 && k < 2);
        }
      }
    }
    BAR();
    MT(3); WAITV(0);
    STAMP_OUT();
  } else {
    const int j = wid - 4;
    f32x4 acc[9][4] = {};
    unsigned e0, e1, e2, e3;
    ASTAMP_DECL();
    {
    LANE_CONSTS();
    const char* WQ = (br ? wqB : wqA) + g * 2048 + j * 128;
    const char* WO = (br ? woB : woA) + (size_t)g * 1024 * 1152;
    const unsigned ring = L_W + j * RG;
    const unsigned voffQ = (lane >> 3) * 8192 + (((lane & 7) ^ ((g4 & 1) << 2)) * 16);
    const unsigned dvo = voffQ ^ ((lane >> 3) * 1152 + j * 256 + (((lane & 7) ^ ((g4 & 1) << 1)) * 16));
    const unsigned pos = lane >> 1;
    const unsigned voffC = (pos ^ (((pos >> 3) & 1) << 2)) * 1152 + 1024 + j * 32 + (lane & 1) * 16;
    const int dOB = (g >> 1) ? -(int)RG : (int)RG;
    f32x16 aq[2][2];
    auto isO = [](int n) -> int { return (int)(n >= 52) | (int)((unsigned)(n - 22) < 8u) | (int)((unsigned)(n - 39) < 8u); };
    auto woff = [&](int n) -> unsigned {
      const int qi = n - (n >= 30 ? 8 : 0) - (n >= 47 ? 8 : 0), u = qi / 9, s = qi - 9 * u;
      const int oi = n >= 52 ? n - 36 : n >= 39 ? n - 31 : n - 22;
      return isO(n) ? (unsigned)(((((oi >> 3) + rot) & 3) * 256 + (oi & 1) * 128 + ((oi & 6) ^ 4) * 16) * 9) | 65536u
                    : (unsigned)(xblk(r0, u, s) * 4096 + ((u + rot) & 3) * 4);
    };
    auto build = [&](int base) -> unsigned {
      const int n = base + lane_id(), m = n + 1;
      const int qi = m - (m >= 30 ? 8 : 0) - (m >= 47 ? 8 : 0), u = qi / 9, s = (qi - 9 * u) & 15;
      const int oi = m >= 52 ? m - 36 : m >= 39 ? m - 31 : m - 22;
      const unsigned boff = isO(m) ? (unsigned)((((oi & 7) >> 1) ^ 2) + (oi & 1) * 28) : (unsigned)((u == 0 ? 0x421032104ull : 0x410321094ull) >> (4 * s)) & 15u;
      const unsigned qw = n == 8 || n == 17 || n == 34 || n == 51;
      const unsigned nba = !(n == 8 || n == 11 || n == 20 || n == 37 || n >= 61);
      const unsigned last = qw || n == 21 || n == 29 || n == 38 || n == 46 || n == 67;
      return woff(n + 2 < 67 ? n + 2 : 67) | boff << 27 | nba << 21 | (unsigned)(n == 12 || n == 59) << 22 | last << 23 | qw << 24 | (unsigned)(n == 51) << 25 | (unsigned)isO(m) << 26;
    };
    u32x4 rs = {0u, 0u, 0x7fffffffu, 0x00020000u};
    unsigned soff, gstep, pstep, ve, vo; unsigned long long cmask;
    auto src = [&](unsigned D) {
      const bool io = (D & 65536u) != 0;
      const unsigned long long bp = (unsigned long long)(io ? WO : WQ);
      rs[0] = (unsigned)bp; rs[1] = (unsigned)(bp >> 32);
      soff = (D & 65535u) << 7; gstep = io ? 128u : 32u * 8192u; pstep = io ? 9216u - 1024u : 65536u - 1024u;
      cmask = io ? ~0ull : 0ull;
      const unsigned msk = io ? ~0u : 0u;
      ve = voffQ ^ (dvo & msk); vo = ve ^ (64u & msk);
      PINS(rs); PINS(soff); PINS(gstep); PINS(pstep); PINS(cmask); PIN(ve); PIN(vo);
    };
#define RF(G, c, half) do { if ((c) == 0) SETM0(ring + (half) + (G) * 4096); bdma<(c) * 1024>(rs, soff + (G) * gstep + (c) * pstep, ((c) & 1) ? vo : ve); } while (0)
#define RFC(half) asm volatile("s_mov_b32 m0, %0\n\ts_mov_b64 exec, %1\n\tbuffer_load_dwordx4 %2, %3, %4 offen lds\n\ts_mov_b64 exec, -1" ::"s"(ring + (half) + 8192), "s"(cmask), "v"(voffC), "s"(rs), "s"(soff) : "memory")
    unsigned ev;
    asm volatile("global_load_dword %0, %1, %2 sc1" : "=v"(ev) : "v"((bid ^ (lane & 3)) * 256), "s"(ws + WS_FLAG) : "memory");
    src(woff(0)); RF(0, 0, 0); RF(0, 1, 0); RF(0, 2, 0); RF(0, 3, 0); RF(1, 0, 0); RF(1, 1, 0); RF(1, 2, 0); RF(1, 3, 0);
    ASTAMP_T(1);
    unsigned tab = build(0);
    PIN(tab); ASTAMP_T(2);
    src(woff(1)); RF(0, 0, 9216); RF(0, 1, 9216); RF(0, 2, 9216); RF(0, 3, 9216);
    {
      GAS unsigned* fl = (GAS unsigned*)(ws + WS_FLAG);
      asm volatile("s_waitcnt vmcnt(12)\n\tv_add_u32 %0, 1, %0" : "+v"(ev)::"memory");
      e0 = __builtin_amdgcn_readlane(ev, 0); e1 = __builtin_amdgcn_readlane(ev, 1); e2 = __builtin_amdgcn_readlane(ev, 2); e3 = __builtin_amdgcn_readlane(ev, 3);
      ABAR();
      if (j == 0 && lane >= 1 && lane < 4) __hip_atomic_store(fl + (bid ^ lane) * 64 + 32 + lane, ev, __ATOMIC_RELAXED, __HIP_MEMORY_SCOPE_AGENT);
    }
    __builtin_amdgcn_s_setprio(2);
    unsigned half = 0, D = 4u << 27 | 1u << 24;
    int ti = 0;
#define XBAR() asm volatile("s_bitcmp0_b32 %0, 22\n\ts_cbranch_scc1 1f\n\ts_barrier\n\ts_barrier\n\ts_barrier\n\ts_barrier\n\ts_barrier\n\ts_barrier\n\ts_barrier\n\ts_barrier\n1:\n\t" \
      "s_bitcmp0_b32 %0, 21\n\ts_cbranch_scc1 2f\n\ts_barrier\n2:" ::"s"(D) : "memory", "scc")
#pragma nounroll
    for (int run = 0; run < 3; ++run) {
      {
#define QA(ks, d) tr2((aa ^ ((d) << 6)) + (ks) * 2048, (aa ^ ((d) << 6)) + (ks) * 2048 + 512)
#define QB(ks, q) lds128((bb ^ ((ks) << 5)) + (q) * 2 * RG)
#define QBB() ((laneQB & 1024u) * ((D >> 27) == 9u ? 8u : 0u) + laneQB + ((D >> 27) << 11))
#define QM1(d, q, fa, fb) do { aq[d][q] = MFMA32(fa, fb, aq[d][q]); SBAR(); } while (0)
        LANE_CONSTS();
        unsigned laneQB = L_H + (r >> 4) * RG + (r & 15) * 128 + ((h ^ ((r >> 1) & 7)) * 16);
        unsigned aa = ring + h * 1024 + q4 * 128 + (((q4 >> 1) & 1) * 64) + gi * 32 + pp * 8 + half, bb = QBB();
        bf16x8 r0_, r1, r2, x0, x1, y0, y1;
        PIN(laneQB); PIN(aa); PIN(bb); SBAR();
        WAITV(8);
        r0_ = QA(0, 0); x0 = QB(0, 0); x1 = QB(0, 1); SBAR(); r1 = QA(0, 1); SBAR(); r2 = QA(1, 0); SBAR(); y0 = QB(1, 0); y1 = QB(1, 1); SBAR();
#define QZ1(d, q, fa, fb, Z) do { aq[d][q] = MFMA32(fa, fb, Z ? f32x16{} : aq[d][q]); SBAR(); } while (0)
#define QBODY(Z) do { \
          const unsigned oh = half ^ 9216u; \
          QZ1(0, 0, r0_, x0, Z); RFC(oh); QZ1(0, 1, r0_, x1, Z); r0_ = QA(1, 1); RF(1, 0, oh); SBAR(); \
          QZ1(1, 0, r1, x0, Z); QZ1(1, 1, r1, x1, Z); RF(1, 1, oh); WAITV(6); r1 = QA(2, 0); x0 = QB(2, 0); x1 = QB(2, 1); SBAR(); \
          QM1(0, 0, r2, y0); RF(1, 2, oh); QM1(0, 1, r2, y1); r2 = QA(3, 0); SBAR(); \
          QM1(1, 0, r0_, y0); RF(1, 3, oh); QM1(1, 1, r0_, y1); r0_ = QA(2, 1); y0 = QB(3, 0); y1 = QB(3, 1); \
          D = __builtin_amdgcn_readlane(tab, ti); ++ti; src(D); SBAR(); \
          QM1(0, 0, r1, x0); RF(0, 0, half); QM1(0, 1, r1, x1); r1 = QA(3, 1); SBAR(); \
          aa += 9216u - 2 * half; PIN(laneQB); bb = QBB(); PIN(aa); PIN(bb); \
          QM1(1, 0, r0_, x0); RF(0, 1, half); QM1(1, 1, r0_, x1); \
          WL(0); WAITL0(); XBAR(); WAITV(6); \
          r0_ = QA(0, 0); x0 = QB(0, 0); x1 = QB(0, 1); SBAR(); \
          QM1(1, 0, r1, y0); RF(0, 2, half); QM1(1, 1, r1, y1); r1 = QA(0, 1); SBAR(); \
          QM1(0, 0, r2, y0); RF(0, 3, half); QM1(0, 1, r2, y1); r2 = QA(1, 0); y0 = QB(1, 0); y1 = QB(1, 1); SBAR(); \
          half ^= 9216u; } while (0)
#pragma nounroll
        do {
          if (D & (1u << 24)) QBODY(true);
#pragma nounroll
          do { QBODY(false); } while (!(D & (1u << 23)));
          if (D & (1u << 24)) {
            const unsigned d = L_H + (j >> 1) * 2 * RG + (j & 1) * 4096 + lane_id() * 16;
#pragma unroll
            for (int d2 = 1; d2 >= 0; --d2)
#pragma unroll
              for (int qt = 0; qt < 2; ++qt) {
                aq[d2][qt] *= SL2E;
                st128(d + qt * RG + d2 * 2048, pack8<0>(aq[d2][qt]));
                st128(d + qt * RG + d2 * 2048 + 1024, pack8<8>(aq[d2][qt]));
              }
            ABAR();
            y0 = QB(1, 0); y1 = QB(1, 1); SBAR();
          }
        } while (!(D & (1u << 26)));
        if (D & (1u << 25)) {
          const int l = lane_id(), o2 = (l < 13 ? l : 13) + 18, o1 = l + 17;
          tab = (unsigned)(((((o2 >> 3) + rot) & 3) * 256 + (o2 & 1) * 128 + ((o2 & 6) ^ 4) * 16) * 9) | 65536u | (unsigned)((((o1 & 7) >> 1) ^ 2) + (o1 & 1) * 28) << 27
                | (unsigned)(l <= 8) << 21 | (unsigned)(l == 7) << 22 | (unsigned)(l == 15) << 23;
          ti = 0; ABAR(); ABAR(); ABAR();
        }
      }
      {
#define OA(t) tr2((aa ^ (((t) & 3) << 5)) + ((t) >> 2) * 4096, (aa ^ (((t) & 3) << 5)) + ((t) >> 2) * 4096 + 512)
#define OM1(t, f, q) acc[t][q] = MFMA16(f, bq[q], acc[t][q])
#define OM2(t, f, q) do { OM1(t, f, q); OM1(t, f, q + 1); SBAR(); } while (0)
#define OM(t, f) do { OM2(t, f, 0); OM2(t, f, 2); } while (0)
        LANE_CONSTS();
        const unsigned laneOC = ring + 8192 + (8 * g4 + 4 * (g4 & 1) + q4) * 32 + p4 * 8;
        const unsigned laneOB = L_H + (g >> 1) * RG + (g4 >> 1) * 1024 + (g4 & 1) * 512 + (g & 1) * 256 + i16 * 16;
        unsigned aa = ring + g4 * 1024 + q4 * 128 + ((((q4 >> 1) & 1) | ((g4 & 1) << 1)) * 32) + p4 * 8 + half;
        unsigned ba = laneOB + ((D >> 27) << 11), bb = ba + dOB;
        bf16x8 bq[4], f0, f1, f2;
        WAITV(8);
        f0 = OA(0); bq[0] = lds128(ba); bq[2] = lds128(bb); bq[1] = lds128(ba ^ 256); f1 = OA(1); bq[3] = lds128(bb ^ 256); f2 = OA(2); SBAR();
#pragma nounroll
        do {
          const unsigned oh = half ^ 9216u;
          OM1(0, f0, 0); SBAR(); RFC(oh); OM1(0, f0, 2); OM1(0, f0, 1); SBAR(); RF(1, 0, oh);
          OM1(1, f1, 0); OM1(1, f1, 2); OM1(1, f1, 1); OM1(0, f0, 3); SBAR(); f0 = OA(3); RF(1, 1, oh); SBAR();
          OM1(1, f1, 3); SBAR(); WAITV(6); f1 = OA(4); SBAR();
          OM2(2, f2, 0); RF(1, 2, oh); OM2(2, f2, 2); f2 = OA(5); SBAR();
          OM2(3, f0, 0); RF(1, 3, oh); OM2(3, f0, 2); f0 = OA(6); D = __builtin_amdgcn_readlane(tab, ti); ++ti; src(D); SBAR();
          OM2(4, f1, 0); RF(0, 0, half); OM2(4, f1, 2); f1 = OA(7); SBAR();
          OM2(5, f2, 0); RF(0, 1, half); OM2(5, f2, 2); { const unsigned ac = laneOC + half; f2 = tr2(ac, ac ^ 128); } SBAR();
          aa += 9216u - 2 * half; ba = laneOB + ((D >> 27) << 11); bb = ba + dOB; PIN(aa); PIN(ba); PIN(bb);
          OM2(6, f0, 0); RF(0, 2, half); OM2(6, f0, 2);
          OM1(7, f1, 0); OM1(7, f1, 2); OM1(8, f2, 0); OM1(8, f2, 2); SBAR();
          WL(0); WAITL0(); XBAR(); WAITV(7);
          f0 = OA(0); bq[0] = lds128(ba); bq[2] = lds128(bb); SBAR();
          OM1(7, f1, 1); SBAR(); RF(0, 3, half); OM1(8, f2, 1); SBAR(); bq[1] = lds128(ba ^ 256); SBAR();
          OM1(7, f1, 3); SBAR(); f1 = OA(1); OM1(8, f2, 3); SBAR(); bq[3] = lds128(bb ^ 256); f2 = OA(2); SBAR();
          half ^= 9216u;
        } while (!(D & (1u << 23)));
      }
    }
    }
    LANE_CONSTS();
    ASTAMP_T(3);
    WAITV(0);
    const __amdgpu_buffer_rsrc_t rs = __builtin_amdgcn_make_buffer_rsrc((void*)(ws + WS_SLAB), 0, (int)WS_SLAB_BYTES, 0x00020000);
#pragma unroll
    for (int i = 1; i < 4; ++i) {
      const int so = (((bid ^ i) * 3 + (i - 1)) * 4 + j) * 4608;
#pragma unroll
      for (int t = 0; t < 8; t += 2) {
        const u32x4 v = {cvtpk(acc[t][i][0], acc[t][i][1]), cvtpk(acc[t][i][2], acc[t][i][3]), cvtpk(acc[t + 1][i][0], acc[t + 1][i][1]), cvtpk(acc[t + 1][i][2], acc[t + 1][i][3])};
        __builtin_amdgcn_raw_buffer_store_b128(v, rs, lane * 16 + t * 512, so, 16);
      }
      const u32x2 v = {cvtpk(acc[8][i][0], acc[8][i][1]), cvtpk(acc[8][i][2], acc[8][i][3])};
      __builtin_amdgcn_raw_buffer_store_b64(v, rs, lane * 8 + 4096, so, 16);
    }
    GAS unsigned* fl = (GAS unsigned*)(ws + WS_FLAG);
    const unsigned want = (lane & 3) == 0 ? e0 : (lane & 3) == 1 ? e1 : (lane & 3) == 2 ? e2 : e3;
    if (j == 0) {
      for (unsigned spins = 0; spins < (1u << 20); ++spins) {
        unsigned v = e0;
        if (lane >= 1 && lane < 4) v = __hip_atomic_load(fl + bid * 64 + 32 + lane, __ATOMIC_RELAXED, __HIP_MEMORY_SCOPE_AGENT);
        if (ALL(v == e0)) break;
        __builtin_amdgcn_s_sleep(1);
      }
    }
    WAITV(0);
    BAR();
    ASTAMP_T(4);
    if (j == 0 && lane == 0) __hip_atomic_store(fl + bid * 64, e0, __ATOMIC_RELAXED, __HIP_MEMORY_SCOPE_AGENT);
    for (unsigned spins = 0; spins < (1u << 20); ++spins) {
      unsigned v = want;
      if (lane >= 1 && lane < 4) v = __hip_atomic_load(fl + (bid ^ lane) * 64, __ATOMIC_RELAXED, __HIP_MEMORY_SCOPE_AGENT);
      if (ALL(v == want)) break;
      __builtin_amdgcn_s_sleep(1);
    }
    __builtin_amdgcn_fence(__ATOMIC_ACQUIRE, "wavefront");
    ASTAMP_T(5);
    u32x4 pv[3][4]; u32x2 pw[3];
#pragma unroll
    for (int i = 1; i < 4; ++i) {
      const int so = ((bid * 3 + (i - 1)) * 4 + j) * 4608;
#pragma unroll
      for (int t = 0; t < 4; ++t) pv[i - 1][t] = __builtin_amdgcn_raw_buffer_load_b128(rs, lane * 16 + t * 1024, so, 16);
      pw[i - 1] = __builtin_amdgcn_raw_buffer_load_b64(rs, lane * 8 + 4096, so, 16);
    }
    char* orow = out + ((size_t)(b * 64 + 16 * g + i16) * 1152 + br * 576 + 4 * g4) * 2;
#pragma unroll
    for (int t = 0; t < 9; ++t) {
      f32x4 v = acc[t][0];
#pragma unroll
      for (int i = 0; i < 3; ++i) {
        const unsigned lo = t < 8 ? pv[i][t >> 1][(t & 1) * 2] : pw[i][0], hi = t < 8 ? pv[i][t >> 1][(t & 1) * 2 + 1] : pw[i][1];
        v[0] += U2F(lo << 16); v[1] += U2F(lo & 0xffff0000u);
        v[2] += U2F(hi << 16); v[3] += U2F(hi & 0xffff0000u);
      }
      const int n0 = t < 8 ? 128 * j + 16 * t : 512 + 16 * j;
      u32x2 pk = {cvtpk(v[0], v[1]), cvtpk(v[2], v[3])};
      *(GAS u32x2*)(orow + n0 * 2) = pk;
    }
    ASTAMP_OUT();
  }
}

#ifdef __HIP_DEVICE_COMPILE__
#pragma clang attribute pop
#endif
extern "C" void kernel_launch(void* const* d_in, const int* in_sizes, int n_in, void* d_out, int out_size, void* d_ws, size_t ws_size,
                              hipStream_t stream) {
  (void)in_sizes; (void)n_in; (void)out_size; (void)ws_size;
  (void)hipFuncSetAttribute((const void*)fused_gqa, hipFuncAttributeMaxDynamicSharedMemorySize, (int)L_TOT);
  hipLaunchKernelGGL(fused_gqa, dim3(256), dim3(512), L_TOT, stream, (const char*)d_in[0], (const char*)d_in[1], (const char*)d_in[2],
                     (const char*)d_in[3], (const char*)d_in[4], (const char*)d_in[5], (const char*)d_in[6], (const char*)d_in[7],
                     (const char*)d_in[8], (const char*)d_in[9], (char*)d_out, (char*)d_ws);
}
```

```cpp
#include <hip/hip_runtime.h>
#include <stdint.h>
#ifdef __HIP_DEVICE_COMPILE__
#pragma clang attribute push(__attribute__((target("no-packed-fp32-ops"))), apply_to = function)
#endif
#define U2F(x) __builtin_bit_cast(float, (unsigned)(x))
#define F2U(x) __builtin_bit_cast(unsigned, (float)(x))
#define ALL(c) (__builtin_amdgcn_ballot_w64(!(c)) == 0)

typedef __attribute__((ext_vector_type(4))) short s16x4;
typedef __attribute__((ext_vector_type(8))) short bf16x8;
typedef __attribute__((ext_vector_type(4))) float f32x4;
typedef __attribute__((ext_vector_type(16))) float f32x16;
typedef __attribute__((ext_vector_type(4))) unsigned u32x4;
typedef __attribute__((ext_vector_type(2))) unsigned u32x2;
#define LAS __attribute__((address_space(3)))
#define GAS __attribute__((address_space(1)))
#define DEVI __device__ __forceinline__

constexpr unsigned L_H = 0;
constexpr unsigned RG = 28672;
constexpr unsigned L_W = 10240;
constexpr unsigned L_KV = 114688;
constexpr unsigned L_TOT = 163840;
constexpr size_t WS_SLAB = 0;
constexpr size_t WS_SLAB_BYTES = (size_t)256 * 3 * 4 * 4608;
constexpr size_t WS_FLAG = (size_t)32 << 20;

constexpr float SL2E = 0.088388347648318447f * 1.4426950408889634f;

DEVI bf16x8 lds128(unsigned a) { return *(LAS bf16x8*)(a); }
DEVI void st128(unsigned a, bf16x8 v) { *(LAS bf16x8*)(a) = v; }
DEVI bf16x8 tr2(unsigned a0, unsigned a1) {
  s16x4 lo = __builtin_amdgcn_ds_read_tr16_b64_v4i16((LAS s16x4*)(a0));
  s16x4 hi = __builtin_amdgcn_ds_read_tr16_b64_v4i16((LAS s16x4*)(a1));
  return (bf16x8){lo[0], lo[1], lo[2], lo[3], hi[0], hi[1], hi[2], hi[3]};
}
typedef __attribute__((ext_vector_type(2))) float f32x2;
typedef __attribute__((ext_vector_type(2))) __bf16 bf16x2;
DEVI unsigned cvtpk(float lo, float hi) { return __builtin_bit_cast(unsigned, __builtin_convertvector((f32x2){lo, hi}, bf16x2)); }
#define PIN(x) asm volatile("" : "+v"(x))
template <int B> DEVI bf16x8 pack8(const f32x16& v) {
  u32x4 w = {cvtpk(v[B], v[B + 1]), cvtpk(v[B + 2], v[B + 3]), cvtpk(v[B + 4], v[B + 5]), cvtpk(v[B + 6], v[B + 7])};
  return __builtin_bit_cast(bf16x8, w);
}
template <int OFF> DEVI void dma_nt2(const char* sb, unsigned voff) {
  asm volatile("global_load_lds_dwordx4 %0, %1 offset:%2 nt" ::"v"(voff), "s"(sb), "i"(OFF) : "memory");
}
template <int OFF> DEVI void dma_nt(unsigned lds, const char* sb, unsigned voff) {
  asm volatile("s_mov_b32 m0, %0\n\ts_nop 0\n\tglobal_load_lds_dwordx4 %1, %2 offset:%3 nt" ::"s"(lds), "v"(voff), "s"(sb), "i"(OFF) : "memory");
}
DEVI void dma(unsigned lds, const char* sb, unsigned voff) {
  asm volatile("s_mov_b32 m0, %0\n\ts_nop 0\n\tglobal_load_lds_dwordx4 %1, %2" ::"s"(lds), "v"(voff), "s"(sb) : "memory");
}
template <int OFF> DEVI void bdma(u32x4 rs, unsigned soff, unsigned voff) {
  asm volatile("buffer_load_dwordx4 %0, %1, %2 offen offset:%3 lds" ::"v"(voff), "s"(rs), "s"(soff), "i"(OFF) : "memory");
}
#define SETM0(x) asm volatile("s_mov_b32 m0, %0\n\ts_nop 0" ::"s"(x) : "memory")
#define PINS(x) asm volatile("" : "+s"(x))
#define WAITV(n) asm volatile("s_waitcnt vmcnt(" #n ")" ::: "memory")
#define WAITL0() asm volatile("s_waitcnt lgkmcnt(0)" ::: "memory")
#define BAR() asm volatile("s_waitcnt lgkmcnt(0)\n\ts_barrier" ::: "memory")
#define SBAR() __builtin_amdgcn_sched_barrier(0)
#define WL(n) __builtin_amdgcn_s_waitcnt(0xC07F | ((n) << 8))
#define MFMA32(a, b, c) __builtin_amdgcn_mfma_f32_32x32x16_bf16(a, b, c, 0, 0, 0)
#define MFMA16(a, b, c) __builtin_amdgcn_mfma_f32_16x16x32_bf16(a, b, c, 0, 0, 0)

DEVI int lane_id() { int l; asm volatile("v_mbcnt_lo_u32_b32 %0, -1, 0\n\tv_mbcnt_hi_u32_b32 %0, -1, %0" : "=v"(l)); return l; }
#define STAMP_DECL()
#define MT(i)
#define MBAR() BAR()
#define LBAR(T) BAR()
#define STAMP_OUT()
#define ASTAMP_DECL()
#define ASTAMP_T(i)
#define ABAR() BAR()
#define AWAITV(n) WAITV(n)
#define ASTAMP_OUT()
DEVI int xblk(int r0, int unit, int s) { int v = r0 + 9 - unit + s; v -= (v >= 18) ? 18 : 0; v -= (v >= 9) ? 9 : 0; return v; }

__global__ __launch_bounds__(512, 2) void fused_gqa(const char* __restrict__ xA, const char* __restrict__ kA, const char* __restrict__ vA,
                                                    const char* __restrict__ wqA, const char* __restrict__ woA, const char* __restrict__ xB,
                                                    const char* __restrict__ kB, const char* __restrict__ vB, const char* __restrict__ wqB,
                                                    const char* __restrict__ woB, char* __restrict__ out, char* __restrict__ ws) {
  extern __shared__ __attribute__((aligned(16))) char shm[];
  const int bid = (int)__builtin_amdgcn_workgroup_id_x(), br = (bid >> 2) & 1, g = bid & 3, b = bid >> 3;
  const int wid = __builtin_amdgcn_readfirstlane((int)__builtin_amdgcn_workitem_id_x() >> 6);
  const int rot = b & 3, r0 = b >> 2;
#define LANE_CONSTS() const int lane = lane_id(); \
  const int r = lane & 31, h = lane >> 5, i16 = lane & 15, q4 = i16 >> 2, p4 = i16 & 3, pp = ((p4 & 1) << 1) | (p4 >> 1), gi = (lane >> 4) & 1, g4 = lane >> 4; \
  (void)r; (void)h; (void)q4; (void)p4; (void)pp; (void)gi; (void)g4;

  if (wid < 4) {
    const int w = wid;
    LANE_CONSTS();
    STAMP_DECL();
    const char* X = (br ? xB : xA) + (size_t)(b * 64 + 16 * w) * 1152;
    const char* Kc = (br ? kB : kA) + (size_t)(b * 16 + 4 * g) * 131072 + w * 4096;
    const char* Vc = (br ? vB : vA) + (size_t)(b * 16 + 4 * g) * 131072 + w * 4096;
    const unsigned laneK = r * 256 + ((h ^ (r & 15)) * 16);
    const unsigned laneV = (4 * h + q4) * 256 + q4 * 64 + gi * 32 + pp * 8;
    const unsigned voffK = g4 * 256 + ((i16 ^ g4) * 16);
    const unsigned voffV = g4 * 256 + ((i16 ^ (g4 << 2)) * 16);
    const unsigned voffX = (lane >> 3) * 1152 + (((lane & 7) ^ g4) * 16);
    const unsigned hb = L_H + w * RG + lane * 16;
    auto kvoff = [&](int idx) -> unsigned { idx = idx < 31 ? idx : 31; return (unsigned)((idx + 8 * rot) & 31) * 16384u; };
    auto issueX = [&](int unit, int s, int slot) {
      const char* p = X + xblk(r0, unit, s) * 128;
      const unsigned d = L_H + w * RG + slot * 2048;
      dma(d, p, voffX); dma(d + (slot == 9 ? 9216 : 1024), p + 9216, voffX ^ 64);
    };
#define KPIECE(c, d, s) do { if ((c) == 0) dma_nt<0>(d, s, voffK); else dma_nt2<(c) * 1024>(s, voffK ^ ((c) * 64)); } while (0)
#define VPIECE(c, d, s) do { if ((c) == 0) dma_nt<0>(d, s, voffV); else dma_nt2<(c) * 1024>(s, voffV); } while (0)
#define RDK(kb, i) lds128(((kb) ^ (((i) >> 1) << 5)) + ((i) & 1) * 8192)
#define RDV(vb, i) tr2(((vb) ^ (((i) & 3) << 6)) + ((i) >> 2) * 4096, ((vb) ^ (((i) & 3) << 6)) + ((i) >> 2) * 4096 + 2048)
    bf16x8 qf[8], fr[8], pk0 = {}, pk1 = {}, pk2, pk3;
    f32x16 o[4] = {}, sa0, sa1, sb0 = {}, sb1 = {}, nmv = {};
    float l = 1.f, alpha = 1.f, mx = 0.f;
    bool resc = false;
#define EXP1(S, i) do { float t_ = S[i]; PIN(t_); t_ = __builtin_amdgcn_exp2f(t_); l += t_; S[i] = t_; } while (0)
#define EXPE(S0, S1, e) do { if ((e) < 16) EXP1(S0, (e) & 15); else EXP1(S1, (e) & 15); } while (0)
#define MAXG(S0, S1, gq) do { \
      if ((gq) == 1) { mx = __builtin_fmaxf(__builtin_fmaxf(S0[0], S0[1]), S0[2]); _Pragma("unroll") for (int i_ = 3; i_ < 13; i_ += 2) mx = __builtin_fmaxf(__builtin_fmaxf(mx, S0[i_]), S0[i_ + 1]); } \
      if ((gq) == 2) { mx = __builtin_fmaxf(__builtin_fmaxf(mx, S0[13]), S0[14]); mx = __builtin_fmaxf(__builtin_fmaxf(mx, S0[15]), S1[0]); _Pragma("unroll") for (int i_ = 1; i_ < 7; i_ += 2) mx = __builtin_fmaxf(__builtin_fmaxf(mx, S1[i_]), S1[i_ + 1]); } \
      if ((gq) == 3) { _Pragma("unroll") for (int i_ = 7; i_ < 15; i_ += 2) mx = __builtin_fmaxf(__builtin_fmaxf(mx, S1[i_]), S1[i_ + 1]); mx = __builtin_fmaxf(mx, S1[15]); } \
      PIN(mx); } while (0)
#define REFUPD(S0, S1, NEWU) do { \
      if (NEWU || __builtin_amdgcn_ballot_w64(mx > 8.f) != 0) { \
        auto rr_ = __builtin_amdgcn_permlane32_swap(F2U(mx), F2U(mx), false, false); \
        float d_ = __builtin_fmaxf(U2F(rr_[0]), U2F(rr_[1])); \
        if (NEWU) { \
          auto r2_ = __builtin_amdgcn_permlane32_swap(F2U(l), F2U(l), false, false); \
          alpha = __builtin_amdgcn_rcpf(U2F(r2_[0]) + U2F(r2_[1])); l = 0.f; \
        } else { d_ = __builtin_fmaxf(d_, 0.f); alpha = __builtin_amdgcn_exp2f(-d_); l *= alpha; resc = true; } \
        const float n_ = (NEWU ? 0.f : nmv[0]) - d_; \
        _Pragma("unroll") for (int i_ = 0; i_ < 16; ++i_) { S0[i_] -= d_; S1[i_] -= d_; nmv[i_] = n_; } \
      } } while (0)
    issueX(0, 0, 4); issueX(0, 1, 0);
    MT(0);
#pragma unroll
    for (int i = 0; i < 4; ++i) st128(L_KV + 16384 + w * 4096 + i * 1024 + lane * 16, bf16x8{});
    WAITV(2); MBAR(); MT(1);
    issueX(0, 2, 1); issueX(0, 3, 2); issueX(0, 4, 3);
    WAITV(6); MBAR();
    issueX(0, 8, 4); WAITV(6); MBAR();
    issueX(0, 5, 0); WAITV(6); MBAR();
    issueX(0, 6, 1); WAITV(6); MBAR();
    issueX(0, 7, 2);
    {
      const unsigned d = L_KV + w * 4096;
      const char* s = Kc + kvoff(0);
      KPIECE(0, d, s); KPIECE(1, d, s); KPIECE(2, d, s); KPIECE(3, d, s);
    }
    issueX(1, 1, 9);
    WAITV(10); MBAR();
    WAITV(8); MBAR();
    WAITV(6); MBAR();
    MBAR();
    WAITV(2);
    MBAR();
    MT(2);
    {
#pragma unroll
      for (int ks = 0; ks < 8; ++ks) qf[ks] = lds128(hb + ks * 1024);
      const char* s = Kc + kvoff(1);
      const unsigned d = L_KV + w * 4096 + 32768;
      KPIECE(0, d, s); KPIECE(1, d, s); KPIECE(2, d, s); KPIECE(3, d, s);
      const unsigned kb = laneK + L_KV;
#pragma unroll
      for (int i = 0; i < 8; ++i) fr[i] = RDK(kb, i);
    }
    unsigned c0 = L_KV, c1 = L_KV + 16384, c2 = L_KV + 32768;
#define ROT() do { const unsigned t_ = c0; c0 = c1; c1 = c2; c2 = t_; } while (0)
#define TICK_A(D0, D1, E0, E1, T, CZ, FIN, XC, XU, XS, XSLOT) do { \
      const unsigned kb_ = laneK + c0, vb_ = laneV + c1, dd_ = c0 + w * 4096; \
      const char* sp_ = Vc + kvoff((T) >> 1); \
      if (!(FIN) && resc) { \
        _Pragma("unroll") for (int e = 0; e < 64; ++e) o[e >> 4][e & 15] *= alpha; \
        resc = false; \
      } \
      SBAR(); \
      _Pragma("unroll") for (int i = 0; i < 16; ++i) { \
        if (i == 0 || i == 4 || i == 8) { if (FIN && i == 4) WL(5); else if (FIN && i == 8) WL(6); else WL(4); SBAR(); } \
        if (i & 1) D1 = MFMA32(fr[i & 7], qf[i >> 1], i < 2 ? (CZ ? f32x16{} : nmv) : D1); else D0 = MFMA32(fr[i & 7], qf[i >> 1], i < 2 ? (CZ ? f32x16{} : nmv) : D0); \
        if (i < 8) fr[i] = RDK(kb_, i + 8); \
        if (i == 10) { \
          SBAR(); if (CZ) { WAITV(8); if (!(XC)) WAITV(4); } else WAITV(4); WL(0); LBAR(T); \
          fr[0] = RDV(vb_, 0); fr[1] = RDV(vb_, 1); fr[2] = RDV(vb_, 2); \
        } \
        if (i > 10) fr[i - 8] = RDV(vb_, i - 8); \
        if (i == 11) VPIECE(0, dd_, sp_); if (i == 12) VPIECE(1, dd_, sp_); if (i == 13) VPIECE(2, dd_, sp_); if (i == 14) VPIECE(3, dd_, sp_); \
        if (i < 9) { EXPE(E0, E1, 14 + 2 * i); EXPE(E0, E1, 15 + 2 * i); PIN(l); } \
        if (i == 1) { pk1 = pack8<8>(E0); PIN(pk1); } \
        if (i == 5) { pk2 = pack8<0>(E1); PIN(pk2); } \
        if (i == 9) { pk3 = pack8<8>(E1); PIN(pk3); } \
        if (i == 15 && (XC)) issueX(XU, XS, XSLOT); \
        if (CZ && (i == 5 || i == 7) && (XC)) issueX(XU, i == 5 ? 2 : 3, i == 5 ? 0 : 1); \
        if (FIN && ((i >= 2 && i <= 8 && !(i & 1)) || (i >= 11 && i <= 14))) { \
          const int c_ = i <= 8 ? 3 + (i >> 1) : i - 11; \
          _Pragma("unroll") for (int e = 0; e < 8; ++e) o[c_ >> 1][(c_ & 1) * 8 + e] *= alpha; \
          st128(hb + c_ * 1024, (c_ & 1) ? pack8<8>(o[c_ >> 1]) : pack8<0>(o[c_ >> 1])); \
        } \
        SBAR(); \
      } \
      ROT(); } while (0)
#define TICK_B(S0, S1, T, newq, NEWU, OZ, XC) do { \
      const unsigned vb_ = laneV + c0, kb_ = laneK + c1, dd_ = c0 + w * 4096; \
      const char* sp_ = Kc + kvoff(((T) + 3) >> 1); \
      _Pragma("unroll") for (int i = 0; i < 16; ++i) { \
        if (i == 0 || i == 4 || i == 8) { if (OZ && i == 0) WL(12); else WL(8); SBAR(); } \
        o[i & 3] = MFMA32(fr[i & 7], (i < 4 ? pk0 : i < 8 ? pk1 : i < 12 ? pk2 : pk3), (OZ && i < 4) ? f32x16{} : o[i & 3]); \
        if (i < 8) fr[i] = RDV(vb_, i + 8); \
        if (i == 10) { \
          SBAR(); WAITV(6); if (!(XC)) WAITV(4); WL(0); LBAR(T); \
          fr[0] = RDK(kb_, 0); fr[1] = RDK(kb_, 1); fr[2] = RDK(kb_, 2); \
        } \
        if (i > 10) fr[i - 8] = RDK(kb_, i - 8); \
        if (i == 11) KPIECE(0, dd_, sp_); if (i == 12) KPIECE(1, dd_, sp_); if (i == 13) KPIECE(2, dd_, sp_); if (i == 14) KPIECE(3, dd_, sp_); \
        if (i >= 1 && i <= 3) MAXG(S0, S1, i); \
        if (i == 4) { SBAR(); REFUPD(S0, S1, NEWU); } \
        if (i >= 5) { \
          constexpr int e0_[12] = {0, 1, 2, 3, 5, 7, 8, 9, 10, 11, 12, 14}; \
          _Pragma("unroll") for (int e = e0_[i - 5]; e < e0_[i - 4]; ++e) EXP1(S0, e); \
          PIN(l); } \
        if (i == 12) { pk0 = pack8<0>(S0); PIN(pk0); } \
        SBAR(); \
      } \
      if (newq) { \
        asm volatile("s_barrier" ::: "memory"); \
        _Pragma("unroll") for (int ks = 0; ks < 8; ++ks) qf[ks] = lds128(hb + ks * 1024); \
      } \
      ROT(); } while (0)
#pragma nounroll
    for (int P = 0; P < 4; ++P) {
      const int T0 = 16 * P;
      TICK_A(sa0, sa1, sb0, sb1, T0, true, false, P < 3, P + 1, 8, 4);
      TICK_B(sa0, sa1, T0 + 1, false, true, false, P < 3);
      TICK_A(sb0, sb1, sa0, sa1, T0 + 2, false, true, false, 0, 0, 0);
      {
        TICK_B(sb0, sb1, T0 + 3, false, false, true, false);
#pragma nounroll
        for (int k = 0; k < 3; ++k) {
          const int T = T0 + 4 + 4 * k;
          const int xu = P + (k == 2 ? 2 : 1);
          TICK_A(sa0, sa1, sb0, sb1, T, false, false, xu < 4, xu, k == 2 ? 1 : 4 + 2 * k, k == 2 ? 9 : 2 - 2 * k);
          TICK_B(sa0, sa1, T + 1, false, false, false, xu < 4);
          TICK_A(sb0, sb1, sa0, sa1, T + 2, false, false, P < 3 && k < 2, xu, 5 + 2 * k, 3 - 2 * k);
          TICK_B(sb0, sb1, T + 3, k == 2 && P < 3, false, false, P < 3 && k < 2);
        }
      }
    }
    {
      const unsigned vb_ = laneV + c1;
      WAITV(4); BAR(); BAR();
#define DI(i) ((((i) & 1) | (((i) & 8) >> 2) | (((i) & 6) << 1)) ^ 2)
#pragma unroll
      for (int i = 0; i < 8; ++i) fr[i] = RDV(vb_, DI(i));
      if (resc) {
#pragma unroll
        for (int e = 0; e < 64; ++e) o[e >> 4][e & 15] *= alpha;
      }
#pragma unroll
      for (int e = 14; e < 32; ++e) EXPE(sb0, sb1, e);
      pk1 = pack8<8>(sb0); pk2 = pack8<0>(sb1); pk3 = pack8<8>(sb1);
      auto r2_ = __builtin_amdgcn_permlane32_swap(F2U(l), F2U(l), false, false);
      alpha = __builtin_amdgcn_rcpf(U2F(r2_[0]) + U2F(r2_[1]));
      SBAR();
#pragma unroll
      for (int i = 0; i < 16; ++i) {
        o[DI(i) & 3] = MFMA32(fr[i & 7], ((i & 6) == 0 ? pk0 : (i & 6) == 2 ? pk1 : (i & 6) == 4 ? pk2 : pk3), o[DI(i) & 3]);
        if (i < 8) fr[i] = RDV(vb_, DI(i + 8));
        if (i >= 9 && (i & 1)) {
          const int c_ = 4 + ((i - 9) >> 1);
#pragma unroll
          for (int e = 0; e < 8; ++e) o[c_ >> 1][(c_ & 1) * 8 + e] *= alpha;
          st128(hb + c_ * 1024, (c_ & 1) ? pack8<8>(o[c_ >> 1]) : pack8<0>(o[c_ >> 1]));
        }
        SBAR();
      }
      BAR();
#pragma unroll
      for (int c_ = 0; c_ < 4; ++c_) {
#pragma unroll
        for (int e = 0; e < 8; ++e) o[c_ >> 1][(c_ & 1) * 8 + e] *= alpha;
        st128(hb + c_ * 1024, (c_ & 1) ? pack8<8>(o[c_ >> 1]) : pack8<0>(o[c_ >> 1]));
      }
      BAR();
    }
    MT(3); WAITV(0);
    STAMP_OUT();
  } else {
    const int j = wid - 4;
    f32x4 acc[9][4] = {};
    unsigned e0, e1, e2, e3;
    ASTAMP_DECL();
    {
    LANE_CONSTS();
    const char* WQ = (br ? wqB : wqA) + g * 2048 + j * 128;
    const char* WO = (br ? woB : woA) + (size_t)g * 1024 * 1152;
    const unsigned ring = L_W + j * RG;
    const unsigned voffQ = (lane >> 3) * 8192 + (((lane & 7) ^ ((g4 & 1) << 2)) * 16);
    const unsigned dvo = voffQ ^ ((lane >> 3) * 1152 + j * 256 + (((lane & 7) ^ ((g4 & 1) << 1)) * 16));
    const unsigned pos = lane >> 1;
    const unsigned voffC = (pos ^ (((pos >> 3) & 1) << 2)) * 1152 + 1024 + j * 32 + (lane & 1) * 16;
    const int dOB = (g >> 1) ? -(int)RG : (int)RG;
    f32x16 aq[2][2];
    auto isO = [](int n) -> int { return (int)(n >= 52) | (int)((unsigned)(n - 22) < 8u) | (int)((unsigned)(n - 39) < 8u); };
    auto woff = [&](int n) -> unsigned {
      const int qi = n - (n >= 30 ? 8 : 0) - (n >= 47 ? 8 : 0), u = qi / 9, s = qi - 9 * u;
      const int oi = n >= 52 ? n - 36 : n >= 39 ? n - 31 : n - 22;
      return isO(n) ? (unsigned)(((((oi >> 3) + rot) & 3) * 256 + (oi & 1) * 128 + ((oi & 6) ^ 4) * 16) * 9) | 65536u
                    : (unsigned)(xblk(r0, u, s) * 4096 + ((u + rot) & 3) * 4);
    };
    auto build = [&](int base) -> unsigned {
      const int n = base + lane_id(), m = n + 1;
      const int qi = m - (m >= 30 ? 8 : 0) - (m >= 47 ? 8 : 0), u = qi / 9, s = (qi - 9 * u) & 15;
      const int oi = m >= 52 ? m - 36 : m >= 39 ? m - 31 : m - 22;
      const unsigned boff = isO(m) ? (unsigned)((((oi & 7) >> 1) ^ 2) + (oi & 1) * 28) : (unsigned)((u == 0 ? 0x421032104ull : 0x410321094ull) >> (4 * s)) & 15u;
      const unsigned qw = n == 8 || n == 17 || n == 34 || n == 51;
      const unsigned nba = !(n == 8 || n == 11 || n == 20 || n == 37 || n >= 61);
      const unsigned last = qw || n == 21 || n == 29 || n == 38 || n == 46 || n == 67;
      return woff(n + 2 < 67 ? n + 2 : 67) | boff << 27 | nba << 21 | (unsigned)(n == 12 || n == 59) << 22 | last << 23 | qw << 24 | (unsigned)(n == 51) << 25 | (unsigned)isO(m) << 26;
    };
    u32x4 rs = {0u, 0u, 0x7fffffffu, 0x00020000u};
    unsigned soff, gstep, pstep, ve, vo; unsigned long long cmask;
    auto src = [&](unsigned D) {
      const bool io = (D & 65536u) != 0;
      const unsigned long long bp = (unsigned long long)(io ? WO : WQ);
      rs[0] = (unsigned)bp; rs[1] = (unsigned)(bp >> 32);
      soff = (D & 65535u) << 7; gstep = io ? 128u : 32u * 8192u; pstep = io ? 9216u - 1024u : 65536u - 1024u;
      cmask = io ? ~0ull : 0ull;
      const unsigned msk = io ? ~0u : 0u;
      ve = voffQ ^ (dvo & msk); vo = ve ^ (64u & msk);
      PINS(rs); PINS(soff); PINS(gstep); PINS(pstep); PINS(cmask); PIN(ve); PIN(vo);
    };
#define RF(G, c, half) do { if ((c) == 0) SETM0(ring + (half) + (G) * 4096); bdma<(c) * 1024>(rs, soff + (G) * gstep + (c) * pstep, ((c) & 1) ? vo : ve); } while (0)
#define RFC(half) asm volatile("s_mov_b32 m0, %0\n\ts_mov_b64 exec, %1\n\tbuffer_load_dwordx4 %2, %3, %4 offen lds\n\ts_mov_b64 exec, -1" ::"s"(ring + (half) + 8192), "s"(cmask), "v"(voffC), "s"(rs), "s"(soff) : "memory")
    unsigned ev;
    asm volatile("global_load_dword %0, %1, %2 sc1" : "=v"(ev) : "v"((bid ^ (lane & 3)) * 256), "s"(ws + WS_FLAG) : "memory");
    src(woff(0)); RF(0, 0, 0); RF(0, 1, 0); RF(0, 2, 0); RF(0, 3, 0); RF(1, 0, 0); RF(1, 1, 0); RF(1, 2, 0); RF(1, 3, 0);
    ASTAMP_T(1);
    unsigned tab = build(0);
    PIN(tab); ASTAMP_T(2);
    src(woff(1)); RF(0, 0, 9216); RF(0, 1, 9216); RF(0, 2, 9216); RF(0, 3, 9216);
    {
      GAS unsigned* fl = (GAS unsigned*)(ws + WS_FLAG);
      asm volatile("s_waitcnt vmcnt(12)\n\tv_add_u32 %0, 1, %0" : "+v"(ev)::"memory");
      e0 = __builtin_amdgcn_readlane(ev, 0); e1 = __builtin_amdgcn_readlane(ev, 1); e2 = __builtin_amdgcn_readlane(ev, 2); e3 = __builtin_amdgcn_readlane(ev, 3);
      ABAR();
      if (j == 0 && lane >= 1 && lane < 4) __hip_atomic_store(fl + (bid ^ lane) * 64 + 32 + lane, ev, __ATOMIC_RELAXED, __HIP_MEMORY_SCOPE_AGENT);
    }
    __builtin_amdgcn_s_setprio(2);
    unsigned half = 0, D = 4u << 27 | 1u << 24;
    int ti = 0;
#define XBAR() asm volatile("s_bitcmp0_b32 %0, 22\n\ts_cbranch_scc1 1f\n\ts_barrier\n\ts_barrier\n\ts_barrier\n\ts_barrier\n\ts_barrier\n\ts_barrier\n\ts_barrier\n\ts_barrier\n1:\n\t" \
      "s_bitcmp0_b32 %0, 21\n\ts_cbranch_scc1 2f\n\ts_barrier\n2:" ::"s"(D) : "memory", "scc")
#pragma nounroll
    for (int run = 0; run < 3; ++run) {
      {
#define QA(ks, d) tr2((aa ^ ((d) << 6)) + (ks) * 2048, (aa ^ ((d) << 6)) + (ks) * 2048 + 512)
#define QB(ks, q) lds128((bb ^ ((ks) << 5)) + (q) * 2 * RG)
#define QBB() ((laneQB & 1024u) * ((D >> 27) == 9u ? 8u : 0u) + laneQB + ((D >> 27) << 11))
#define QM1(d, q, fa, fb) do { aq[d][q] = MFMA32(fa, fb, aq[d][q]); SBAR(); } while (0)
        LANE_CONSTS();
        unsigned laneQB = L_H + (r >> 4) * RG + (r & 15) * 128 + ((h ^ ((r >> 1) & 7)) * 16);
        unsigned aa = ring + h * 1024 + q4 * 128 + (((q4 >> 1) & 1) * 64) + gi * 32 + pp * 8 + half, bb = QBB();
        bf16x8 r0_, r1, r2, x0, x1, y0, y1;
        PIN(laneQB); PIN(aa); PIN(bb); SBAR();
        WAITV(8);
        r0_ = QA(0, 0); x0 = QB(0, 0); x1 = QB(0, 1); SBAR(); r1 = QA(0, 1); SBAR(); r2 = QA(1, 0); SBAR(); y0 = QB(1, 0); y1 = QB(1, 1); SBAR();
#define QZ1(d, q, fa, fb, Z) do { aq[d][q] = MFMA32(fa, fb, Z ? f32x16{} : aq[d][q]); SBAR(); } while (0)
#define QBODY(Z) do { \
          const unsigned oh = half ^ 9216u; \
          QZ1(0, 0, r0_, x0, Z); RFC(oh); QZ1(0, 1, r0_, x1, Z); r0_ = QA(1, 1); RF(1, 0, oh); SBAR(); \
          QZ1(1, 0, r1, x0, Z); QZ1(1, 1, r1, x1, Z); RF(1, 1, oh); WAITV(6); r1 = QA(2, 0); x0 = QB(2, 0); x1 = QB(2, 1); SBAR(); \
          QM1(0, 0, r2, y0); RF(1, 2, oh); QM1(0, 1, r2, y1); r2 = QA(3, 0); SBAR(); \
          QM1(1, 0, r0_, y0); RF(1, 3, oh); QM1(1, 1, r0_, y1); r0_ = QA(2, 1); y0 = QB(3, 0); y1 = QB(3, 1); \
          D = __builtin_amdgcn_readlane(tab, ti); ++ti; src(D); SBAR(); \
          QM1(0, 0, r1, x0); RF(0, 0, half); QM1(0, 1, r1, x1); r1 = QA(3, 1); SBAR(); \
          aa += 9216u - 2 * half; PIN(laneQB); bb = QBB(); PIN(aa); PIN(bb); \
          QM1(1, 0, r0_, x0); RF(0, 1, half); QM1(1, 1, r0_, x1); \
          WL(0); WAITL0(); XBAR(); WAITV(6); \
          r0_ = QA(0, 0); x0 = QB(0, 0); x1 = QB(0, 1); SBAR(); \
          QM1(1, 0, r1, y0); RF(0, 2, half); QM1(1, 1, r1, y1); r1 = QA(0, 1); SBAR(); \
          QM1(0, 0, r2, y0); RF(0, 3, half); QM1(0, 1, r2, y1); r2 = QA(1, 0); y0 = QB(1, 0); y1 = QB(1, 1); SBAR(); \
          half ^= 9216u; } while (0)
#pragma nounroll
        do {
          if (D & (1u << 24)) QBODY(true);
#pragma nounroll
          do { QBODY(false); } while (!(D & (1u << 23)));
          if (D & (1u << 24)) {
            const unsigned d = L_H + (j >> 1) * 2 * RG + (j & 1) * 4096 + lane_id() * 16;
#pragma unroll
            for (int d2 = 1; d2 >= 0; --d2)
#pragma unroll
              for (int qt = 0; qt < 2; ++qt) {
                aq[d2][qt] *= SL2E;
                st128(d + qt * RG + d2 * 2048, pack8<0>(aq[d2][qt]));
                st128(d + qt * RG + d2 * 2048 + 1024, pack8<8>(aq[d2][qt]));
              }
            ABAR();
            y0 = QB(1, 0); y1 = QB(1, 1); SBAR();
          }
        } while (!(D & (1u << 26)));
        if (D & (1u << 25)) {
          const int l = lane_id(), o2 = (l < 13 ? l : 13) + 18, o1 = l + 17;
          tab = (unsigned)(((((o2 >> 3) + rot) & 3) * 256 + (o2 & 1) * 128 + ((o2 & 6) ^ 4) * 16) * 9) | 65536u | (unsigned)((((o1 & 7) >> 1) ^ 2) + (o1 & 1) * 28) << 27
                | (unsigned)(l <= 8) << 21 | (unsigned)(l == 7) << 22 | (unsigned)(l == 15) << 23;
          ti = 0; ABAR(); ABAR(); ABAR();
        }
      }
      {
#define OA(t) tr2((aa ^ (((t) & 3) << 5)) + ((t) >> 2) * 4096, (aa ^ (((t) & 3) << 5)) + ((t) >> 2) * 4096 + 512)
#define OM1(t, f, q) acc[t][q] = MFMA16(f, bq[q], acc[t][q])
#define OM2(t, f, q) do { OM1(t, f, q); OM1(t, f, q + 1); SBAR(); } while (0)
#define OM(t, f) do { OM2(t, f, 0); OM2(t, f, 2); } while (0)
        LANE_CONSTS();
        const unsigned laneOC = ring + 8192 + (8 * g4 + 4 * (g4 & 1) + q4) * 32 + p4 * 8;
        const unsigned laneOB = L_H + (g >> 1) * RG + (g4 >> 1) * 1024 + (g4 & 1) * 512 + (g & 1) * 256 + i16 * 16;
        unsigned aa = ring + g4 * 1024 + q4 * 128 + ((((q4 >> 1) & 1) | ((g4 & 1) << 1)) * 32) + p4 * 8 + half;
        unsigned ba = laneOB + ((D >> 27) << 11), bb = ba + dOB;
        bf16x8 bq[4], f0, f1, f2;
        WAITV(8);
        f0 = OA(0); bq[0] = lds128(ba); bq[2] = lds128(bb); bq[1] = lds128(ba ^ 256); f1 = OA(1); bq[3] = lds128(bb ^ 256); f2 = OA(2); SBAR();
#pragma nounroll
        do {
          const unsigned oh = half ^ 9216u;
          OM1(0, f0, 0); SBAR(); RFC(oh); OM1(0, f0, 2); OM1(0, f0, 1); SBAR(); RF(1, 0, oh);
          OM1(1, f1, 0); OM1(1, f1, 2); OM1(1, f1, 1); OM1(0, f0, 3); SBAR(); f0 = OA(3); RF(1, 1, oh); SBAR();
          OM1(1, f1, 3); SBAR(); WAITV(6); f1 = OA(4); SBAR();
          OM2(2, f2, 0); RF(1, 2, oh); OM2(2, f2, 2); f2 = OA(5); SBAR();
          OM2(3, f0, 0); RF(1, 3, oh); OM2(3, f0, 2); f0 = OA(6); D = __builtin_amdgcn_readlane(tab, ti); ++ti; src(D); SBAR();
          OM2(4, f1, 0); RF(0, 0, half); OM2(4, f1, 2); f1 = OA(7); SBAR();
          OM2(5, f2, 0); RF(0, 1, half); OM2(5, f2, 2); { const unsigned ac = laneOC + half; f2 = tr2(ac, ac ^ 128); } SBAR();
          aa += 9216u - 2 * half; ba = laneOB + ((D >> 27) << 11); bb = ba + dOB; PIN(aa); PIN(ba); PIN(bb);
          OM2(6, f0, 0); RF(0, 2, half); OM2(6, f0, 2);
          OM1(7, f1, 0); OM1(7, f1, 2); OM1(8, f2, 0); OM1(8, f2, 2); SBAR();
          WL(0); WAITL0(); XBAR(); WAITV(7);
          f0 = OA(0); bq[0] = lds128(ba); bq[2] = lds128(bb); SBAR();
          OM1(7, f1, 1); SBAR(); RF(0, 3, half); OM1(8, f2, 1); SBAR(); bq[1] = lds128(ba ^ 256); SBAR();
          OM1(7, f1, 3); SBAR(); f1 = OA(1); OM1(8, f2, 3); SBAR(); bq[3] = lds128(bb ^ 256); f2 = OA(2); SBAR();
          half ^= 9216u;
        } while (!(D & (1u << 23)));
      }
    }
    }
    LANE_CONSTS();
    ASTAMP_T(3);
    const __amdgpu_buffer_rsrc_t rs = __builtin_amdgcn_make_buffer_rsrc((void*)(ws + WS_SLAB), 0, (int)WS_SLAB_BYTES, 0x00020000);
#pragma unroll
    for (int i = 1; i < 4; ++i) {
      const int so = (((bid ^ i) * 3 + (i - 1)) * 4 + j) * 4608;
#pragma unroll
      for (int t = 0; t < 8; t += 2) {
        const u32x4 v = {cvtpk(acc[t][i][0], acc[t][i][1]), cvtpk(acc[t][i][2], acc[t][i][3]), cvtpk(acc[t + 1][i][0], acc[t + 1][i][1]), cvtpk(acc[t + 1][i][2], acc[t + 1][i][3])};
        __builtin_amdgcn_raw_buffer_store_b128(v, rs, lane * 16 + t * 512, so, 16);
      }
      const u32x2 v = {cvtpk(acc[8][i][0], acc[8][i][1]), cvtpk(acc[8][i][2], acc[8][i][3])};
      __builtin_amdgcn_raw_buffer_store_b64(v, rs, lane * 8 + 4096, so, 16);
    }
    GAS unsigned* fl = (GAS unsigned*)(ws + WS_FLAG);
    const unsigned want = (lane & 3) == 0 ? e0 : (lane & 3) == 1 ? e1 : (lane & 3) == 2 ? e2 : e3;
    if (j == 0) {
      for (unsigned spins = 0; spins < (1u << 20); ++spins) {
        unsigned v = e0;
        if (lane >= 1 && lane < 4) v = __hip_atomic_load(fl + bid * 64 + 32 + lane, __ATOMIC_RELAXED, __HIP_MEMORY_SCOPE_AGENT);
        if (ALL(v == e0)) break;
        __builtin_amdgcn_s_sleep(1);
      }
    }
    WAITV(0);
    BAR();
    ASTAMP_T(4);
    if (j == 0 && lane == 0) __hip_atomic_store(fl + bid * 64, e0, __ATOMIC_RELAXED, __HIP_MEMORY_SCOPE_AGENT);
    for (unsigned spins = 0; spins < (1u << 20); ++spins) {
      unsigned v = want;
      if (lane >= 1 && lane < 4) v = __hip_atomic_load(fl + (bid ^ lane) * 64, __ATOMIC_RELAXED, __HIP_MEMORY_SCOPE_AGENT);
      if (ALL(v == want)) break;
      __builtin_amdgcn_s_sleep(1);
    }
    __builtin_amdgcn_fence(__ATOMIC_ACQUIRE, "wavefront");
    ASTAMP_T(5);
    u32x4 pv[3][4]; u32x2 pw[3];
#pragma unroll
    for (int i = 1; i < 4; ++i) {
      const int so = ((bid * 3 + (i - 1)) * 4 + j) * 4608;
#pragma unroll
      for (int t = 0; t < 4; ++t) pv[i - 1][t] = __builtin_amdgcn_raw_buffer_load_b128(rs, lane * 16 + t * 1024, so, 16);
      pw[i - 1] = __builtin_amdgcn_raw_buffer_load_b64(rs, lane * 8 + 4096, so, 16);
    }
    char* orow = out + ((size_t)(b * 64 + 16 * g + i16) * 1152 + br * 576) * 2;
    unsigned c0_ = 0x00003f80u, c1_ = 0x3f800000u; PIN(c0_); PIN(c1_);
    const bf16x2 sel0 = __builtin_bit_cast(bf16x2, c0_), sel1 = __builtin_bit_cast(bf16x2, c1_);
    u32x2 pk[9];
#pragma unroll
    for (int t = 0; t < 9; ++t) {
      f32x4 v = acc[t][0];
#pragma unroll
      for (int i = 0; i < 3; ++i) {
        const bf16x2 lo = __builtin_bit_cast(bf16x2, t < 8 ? pv[i][t >> 1][(t & 1) * 2] : pw[i][0]), hi = __builtin_bit_cast(bf16x2, t < 8 ? pv[i][t >> 1][(t & 1) * 2 + 1] : pw[i][1]);
        v[0] = __builtin_amdgcn_fdot2_f32_bf16(lo, sel0, v[0], false); v[1] = __builtin_amdgcn_fdot2_f32_bf16(lo, sel1, v[1], false);
        v[2] = __builtin_amdgcn_fdot2_f32_bf16(hi, sel0, v[2], false); v[3] = __builtin_amdgcn_fdot2_f32_bf16(hi, sel1, v[3], false);
      }
      pk[t] = (u32x2){cvtpk(v[0], v[1]), cvtpk(v[2], v[3])};
      if (t & 1) {
        auto s0 = __builtin_amdgcn_permlane16_swap(pk[t - 1][0], pk[t][0], false, false);
        auto s1 = __builtin_amdgcn_permlane16_swap(pk[t - 1][1], pk[t][1], false, false);
        *(GAS u32x4*)(orow + (128 * j + 16 * (t - 1) + 16 * (g4 & 1) + 8 * (g4 >> 1)) * 2) = (u32x4){s0[0], s1[0], s0[1], s1[1]};
      }
    }
    *(GAS u32x2*)(orow + (512 + 16 * j + 4 * g4) * 2) = pk[8];
    ASTAMP_OUT();
  }
}

#ifdef __HIP_DEVICE_COMPILE__
#pragma clang attribute pop
#endif
extern "C" void kernel_launch(void* const* d_in, const int* in_sizes, int n_in, void* d_out, int out_size, void* d_ws, size_t ws_size,
                              hipStream_t stream) {
  (void)in_sizes; (void)n_in; (void)out_size; (void)ws_size;
  (void)hipFuncSetAttribute((const void*)fused_gqa, hipFuncAttributeMaxDynamicSharedMemorySize, (int)L_TOT);
  hipLaunchKernelGGL(fused_gqa, dim3(256), dim3(512), L_TOT, stream, (const char*)d_in[0], (const char*)d_in[1], (const char*)d_in[2],
                     (const char*)d_in[3], (const char*)d_in[4], (const char*)d_in[5], (const char*)d_in[6], (const char*)d_in[7],
                     (const char*)d_in[8], (const char*)d_in[9], (char*)d_out, (char*)d_ws);
}
```

```cpp
#include <hip/hip_runtime.h>
#include <stdint.h>
#ifdef __HIP_DEVICE_COMPILE__
#pragma clang attribute push(__attribute__((target("no-packed-fp32-ops"))), apply_to = function)
#endif
#define U2F(x) __builtin_bit_cast(float, (unsigned)(x))
#define F2U(x) __builtin_bit_cast(unsigned, (float)(x))
#define ALL(c) (__builtin_amdgcn_ballot_w64(!(c)) == 0)

typedef __attribute__((ext_vector_type(4))) short s16x4;
typedef __attribute__((ext_vector_type(8))) short bf16x8;
typedef __attribute__((ext_vector_type(4))) float f32x4;
typedef __attribute__((ext_vector_type(16))) float f32x16;
typedef __attribute__((ext_vector_type(4))) unsigned u32x4;
typedef __attribute__((ext_vector_type(2))) unsigned u32x2;
#define LAS __attribute__((address_space(3)))
#define GAS __attribute__((address_space(1)))
#define DEVI __device__ __forceinline__

constexpr unsigned L_H = 0;
constexpr unsigned RG = 28672;
constexpr unsigned L_W = 10240;
constexpr unsigned L_KV = 114688;
constexpr unsigned L_TOT = 163840;
constexpr size_t WS_SLAB = 0;
constexpr size_t WS_SLAB_BYTES = (size_t)256 * 3 * 4 * 4608;
constexpr size_t WS_FLAG = (size_t)32 << 20;

constexpr float SL2E = 0.088388347648318447f * 1.4426950408889634f;

DEVI bf16x8 lds128(unsigned a) { return *(LAS bf16x8*)(a); }
DEVI void st128(unsigned a, bf16x8 v) { *(LAS bf16x8*)(a) = v; }
DEVI bf16x8 tr2(unsigned a0, unsigned a1) {
  s16x4 lo = __builtin_amdgcn_ds_read_tr16_b64_v4i16((LAS s16x4*)(a0));
  s16x4 hi = __builtin_amdgcn_ds_read_tr16_b64_v4i16((LAS s16x4*)(a1));
  return (bf16x8){lo[0], lo[1], lo[2], lo[3], hi[0], hi[1], hi[2], hi[3]};
}
typedef __attribute__((ext_vector_type(2))) float f32x2;
typedef __attribute__((ext_vector_type(2))) __bf16 bf16x2;
DEVI unsigned cvtpk(float lo, float hi) { return __builtin_bit_cast(unsigned, __builtin_convertvector((f32x2){lo, hi}, bf16x2)); }
#define PIN(x) asm volatile("" : "+v"(x))
template <int B> DEVI bf16x8 pack8(const f32x16& v) {
  u32x4 w = {cvtpk(v[B], v[B + 1]), cvtpk(v[B + 2], v[B + 3]), cvtpk(v[B + 4], v[B + 5]), cvtpk(v[B + 6], v[B + 7])};
  return __builtin_bit_cast(bf16x8, w);
}
template <int OFF> DEVI void dma_nt2(const char* sb, unsigned voff) {
  asm volatile("global_load_lds_dwordx4 %0, %1 offset:%2 nt" ::"v"(voff), "s"(sb), "i"(OFF) : "memory");
}
template <int OFF> DEVI void dma_nt(unsigned lds, const char* sb, unsigned voff) {
  asm volatile("s_mov_b32 m0, %0\n\ts_nop 0\n\tglobal_load_lds_dwordx4 %1, %2 offset:%3 nt" ::"s"(lds), "v"(voff), "s"(sb), "i"(OFF) : "memory");
}
DEVI void dma(unsigned lds, const char* sb, unsigned voff) {
  asm volatile("s_mov_b32 m0, %0\n\ts_nop 0\n\tglobal_load_lds_dwordx4 %1, %2" ::"s"(lds), "v"(voff), "s"(sb) : "memory");
}
template <int OFF> DEVI void bdma(u32x4 rs, unsigned soff, unsigned voff) {
  asm volatile("buffer_load_dwordx4 %0, %1, %2 offen offset:%3 lds" ::"v"(voff), "s"(rs), "s"(soff), "i"(OFF) : "memory");
}
#define SETM0(x) asm volatile("s_mov_b32 m0, %0\n\ts_nop 0" ::"s"(x) : "memory")
#define PINS(x) asm volatile("" : "+s"(x))
#define WAITV(n) asm volatile("s_waitcnt vmcnt(" #n ")" ::: "memory")
#define WAITL0() asm volatile("s_waitcnt lgkmcnt(0)" ::: "memory")
#define BAR() asm volatile("s_waitcnt lgkmcnt(0)\n\ts_barrier" ::: "memory")
#define SBAR() __builtin_amdgcn_sched_barrier(0)
#define WL(n) __builtin_amdgcn_s_waitcnt(0xC07F | ((n) << 8))
#define MFMA32(a, b, c) __builtin_amdgcn_mfma_f32_32x32x16_bf16(a, b, c, 0, 0, 0)
#define MFMA16(a, b, c) __builtin_amdgcn_mfma_f32_16x16x32_bf16(a, b, c, 0, 0, 0)

DEVI int lane_id() { int l; asm volatile("v_mbcnt_lo_u32_b32 %0, -1, 0\n\tv_mbcnt_hi_u32_b32 %0, -1, %0" : "=v"(l)); return l; }
#define STAMP_DECL()
#define MT(i)
#define MBAR() BAR()
#define LBAR(T) BAR()
#define STAMP_OUT()
#define ASTAMP_DECL()
#define ASTAMP_T(i)
#define ABAR() BAR()
#define AWAITV(n) WAITV(n)
#define ASTAMP_OUT()
DEVI int xblk(int r0, int unit, int s) { int v = r0 + 9 - unit + s; v -= (v >= 18) ? 18 : 0; v -= (v >= 9) ? 9 : 0; return v; }

__global__ __launch_bounds__(512, 2) void fused_gqa(const char* __restrict__ xA, const char* __restrict__ kA, const char* __restrict__ vA,
                                                    const char* __restrict__ wqA, const char* __restrict__ woA, const char* __restrict__ xB,
                                                    const char* __restrict__ kB, const char* __restrict__ vB, const char* __restrict__ wqB,
                                                    const char* __restrict__ woB, char* __restrict__ out, char* __restrict__ ws) {
  extern __shared__ __attribute__((aligned(16))) char shm[];
  const int bid = (int)__builtin_amdgcn_workgroup_id_x(), br = (bid >> 2) & 1, g = bid & 3, b = bid >> 3;
  const int wid = __builtin_amdgcn_readfirstlane((int)__builtin_amdgcn_workitem_id_x() >> 6);
  const int rot = 0, r0 = b % 9;
#define LANE_CONSTS() const int lane = lane_id(); \
  const int r = lane & 31, h = lane >> 5, i16 = lane & 15, q4 = i16 >> 2, p4 = i16 & 3, pp = ((p4 & 1) << 1) | (p4 >> 1), gi = (lane >> 4) & 1, g4 = lane >> 4; \
  (void)r; (void)h; (void)q4; (void)p4; (void)pp; (void)gi; (void)g4;

  if (wid < 4) {
    const int w = wid;
    LANE_CONSTS();
    STAMP_DECL();
    const char* X = (br ? xB : xA) + (size_t)(b * 64 + 16 * w) * 1152;
    const char* Kc = (br ? kB : kA) + (size_t)(b * 16 + 4 * g) * 131072 + w * 4096;
    const char* Vc = (br ? vB : vA) + (size_t)(b * 16 + 4 * g) * 131072 + w * 4096;
    const unsigned laneK = r * 256 + ((h ^ (r & 15)) * 16);
    const unsigned laneV = (4 * h + q4) * 256 + q4 * 64 + gi * 32 + pp * 8;
    const unsigned voffK = g4 * 256 + ((i16 ^ g4) * 16);
    const unsigned voffV = g4 * 256 + ((i16 ^ (g4 << 2)) * 16);
    const unsigned voffX = (lane >> 3) * 1152 + (((lane & 7) ^ g4) * 16);
    const unsigned hb = L_H + w * RG + lane * 16;
    auto kvoff = [&](int idx) -> unsigned { idx = idx < 31 ? idx : 31; return (unsigned)((idx + 8 * rot) & 31) * 16384u; };
    auto issueX = [&](int unit, int s, int slot) {
      const char* p = X + xblk(r0, unit, s) * 128;
      const unsigned d = L_H + w * RG + slot * 2048;
      dma(d, p, voffX); dma(d + (slot == 9 ? 9216 : 1024), p + 9216, voffX ^ 64);
    };
#define KPIECE(c, d, s) do { if ((c) == 0) dma_nt<0>(d, s, voffK); else dma_nt2<(c) * 1024>(s, voffK ^ ((c) * 64)); } while (0)
#define VPIECE(c, d, s) do { if ((c) == 0) dma_nt<0>(d, s, voffV); else dma_nt2<(c) * 1024>(s, voffV); } while (0)
#define RDK(kb, i) lds128(((kb) ^ (((i) >> 1) << 5)) + ((i) & 1) * 8192)
#define RDV(vb, i) tr2(((vb) ^ (((i) & 3) << 6)) + ((i) >> 2) * 4096, ((vb) ^ (((i) & 3) << 6)) + ((i) >> 2) * 4096 + 2048)
    bf16x8 qf[8], fr[8], pk0 = {}, pk1 = {}, pk2, pk3;
    f32x16 o[4] = {}, sa0, sa1, sb0 = {}, sb1 = {}, nmv = {};
    float l = 1.f, alpha = 1.f, mx = 0.f;
    bool resc = false;
#define EXP1(S, i) do { float t_ = S[i]; PIN(t_); t_ = __builtin_amdgcn_exp2f(t_); l += t_; S[i] = t_; } while (0)
#define EXPE(S0, S1, e) do { if ((e) < 16) EXP1(S0, (e) & 15); else EXP1(S1, (e) & 15); } while (0)
#define MAXG(S0, S1, gq) do { \
      if ((gq) == 1) { mx = __builtin_fmaxf(__builtin_fmaxf(S0[0], S0[1]), S0[2]); _Pragma("unroll") for (int i_ = 3; i_ < 13; i_ += 2) mx = __builtin_fmaxf(__builtin_fmaxf(mx, S0[i_]), S0[i_ + 1]); } \
      if ((gq) == 2) { mx = __builtin_fmaxf(__builtin_fmaxf(mx, S0[13]), S0[14]); mx = __builtin_fmaxf(__builtin_fmaxf(mx, S0[15]), S1[0]); _Pragma("unroll") for (int i_ = 1; i_ < 7; i_ += 2) mx = __builtin_fmaxf(__builtin_fmaxf(mx, S1[i_]), S1[i_ + 1]); } \
      if ((gq) == 3) { _Pragma("unroll") for (int i_ = 7; i_ < 15; i_ += 2) mx = __builtin_fmaxf(__builtin_fmaxf(mx, S1[i_]), S1[i_ + 1]); mx = __builtin_fmaxf(mx, S1[15]); } \
      PIN(mx); } while (0)
#define REFUPD(S0, S1, NEWU) do { \
      if (NEWU || __builtin_amdgcn_ballot_w64(mx > 8.f) != 0) { \
        auto rr_ = __builtin_amdgcn_permlane32_swap(F2U(mx), F2U(mx), false, false); \
        float d_ = __builtin_fmaxf(U2F(rr_[0]), U2F(rr_[1])); \
        if (NEWU) { \
          auto r2_ = __builtin_amdgcn_permlane32_swap(F2U(l), F2U(l), false, false); \
          alpha = __builtin_amdgcn_rcpf(U2F(r2_[0]) + U2F(r2_[1])); l = 0.f; \
        } else { d_ = __builtin_fmaxf(d_, 0.f); alpha = __builtin_amdgcn_exp2f(-d_); l *= alpha; resc = true; } \
        const float n_ = (NEWU ? 0.f : nmv[0]) - d_; \
        _Pragma("unroll") for (int i_ = 0; i_ < 16; ++i_) { S0[i_] -= d_; S1[i_] -= d_; nmv[i_] = n_; } \
      } } while (0)
    issueX(0, 0, 4); issueX(0, 1, 0);
    MT(0);
#pragma unroll
    for (int i = 0; i < 4; ++i) st128(L_KV + 16384 + w * 4096 + i * 1024 + lane * 16, bf16x8{});
    WAITV(2); MBAR(); MT(1);
    issueX(0, 2, 1); issueX(0, 3, 2); issueX(0, 4, 3);
    WAITV(6); MBAR();
    unsigned pf = 0;
    const unsigned pt = (unsigned)((b * 4 + w) * 64 + lane);
    const char* wq_ = (br ? wqB : wqA) + g * 2048 + 512;
    const char* wo_ = (br ? woB : woA) + (size_t)g * 1024 * 1152;
    auto pfq = [&](unsigned i) {
      unsigned c = pt + 8192u * i; c = c < 13823u ? c : 13823u;
      const unsigned u = (c >= 4608u) + (c >= 9216u), rem = c - u * 4608u;
      asm volatile("global_load_dword %0, %1, %2" : "+v"(pf) : "v"(u * 512u + (rem >> 3) * 8192u + (rem & 7u) * 64u), "s"(wq_) : "memory");
    };
    auto pfo = [&](unsigned i) {
      unsigned c = pt + 8192u * i; c = c < 18431u ? c : 18431u;
      asm volatile("global_load_dword %0, %1, %2" : "+v"(pf) : "v"(c * 64u), "s"(wo_) : "memory");
    };
    issueX(0, 8, 4); pfq(0); WAITV(7); MBAR();
    issueX(0, 5, 0); pfq(1); WAITV(8); MBAR();
    issueX(0, 6, 1); pfo(0); WAITV(9); MBAR();
    issueX(0, 7, 2);
    {
      const unsigned d = L_KV + w * 4096;
      const char* s = Kc + kvoff(0);
      KPIECE(0, d, s); KPIECE(1, d, s); KPIECE(2, d, s); KPIECE(3, d, s);
    }
    issueX(1, 1, 9);
    pfo(1); WAITV(13); MBAR();
    pfo(2); WAITV(11); MBAR();
    WAITV(8); MBAR();
    MBAR();
    WAITV(0); asm volatile("" ::"v"(pf));
    MBAR();
    MT(2);
    {
#pragma unroll
      for (int ks = 0; ks < 8; ++ks) qf[ks] = lds128(hb + ks * 1024);
      const char* s = Kc + kvoff(1);
      const unsigned d = L_KV + w * 4096 + 32768;
      KPIECE(0, d, s); KPIECE(1, d, s); KPIECE(2, d, s); KPIECE(3, d, s);
      const unsigned kb = laneK + L_KV;
#pragma unroll
      for (int i = 0; i < 8; ++i) fr[i] = RDK(kb, i);
    }
    unsigned c0 = L_KV, c1 = L_KV + 16384, c2 = L_KV + 32768;
#define ROT() do { const unsigned t_ = c0; c0 = c1; c1 = c2; c2 = t_; } while (0)
#define TICK_A(D0, D1, E0, E1, T, CZ, FIN, XC, XU, XS, XSLOT) do { \
      const unsigned kb_ = laneK + c0, vb_ = laneV + c1, dd_ = c0 + w * 4096; \
      const char* sp_ = Vc + kvoff((T) >> 1); \
      if (!(FIN) && resc) { \
        _Pragma("unroll") for (int e = 0; e < 64; ++e) o[e >> 4][e & 15] *= alpha; \
        resc = false; \
      } \
      SBAR(); \
      _Pragma("unroll") for (int i = 0; i < 16; ++i) { \
        if (i == 0 || i == 4 || i == 8) { if (FIN && i == 4) WL(5); else if (FIN && i == 8) WL(6); else WL(4); SBAR(); } \
        if (i & 1) D1 = MFMA32(fr[i & 7], qf[i >> 1], i < 2 ? (CZ ? f32x16{} : nmv) : D1); else D0 = MFMA32(fr[i & 7], qf[i >> 1], i < 2 ? (CZ ? f32x16{} : nmv) : D0); \
        if (i < 8) fr[i] = RDK(kb_, i + 8); \
        if (i == 10) { \
          SBAR(); if (CZ) { WAITV(8); if (!(XC)) WAITV(4); } else WAITV(4); WL(0); LBAR(T); \
          fr[0] = RDV(vb_, 0); fr[1] = RDV(vb_, 1); fr[2] = RDV(vb_, 2); \
        } \
        if (i > 10) fr[i - 8] = RDV(vb_, i - 8); \
        if (i == 11) VPIECE(0, dd_, sp_); if (i == 12) VPIECE(1, dd_, sp_); if (i == 13) VPIECE(2, dd_, sp_); if (i == 14) VPIECE(3, dd_, sp_); \
        if (i < 9) { EXPE(E0, E1, 14 + 2 * i); EXPE(E0, E1, 15 + 2 * i); PIN(l); } \
        if (i == 1) { pk1 = pack8<8>(E0); PIN(pk1); } \
        if (i == 5) { pk2 = pack8<0>(E1); PIN(pk2); } \
        if (i == 9) { pk3 = pack8<8>(E1); PIN(pk3); } \
        if (i == 15 && (XC)) issueX(XU, XS, XSLOT); \
        if (CZ && (i == 5 || i == 7) && (XC)) issueX(XU, i == 5 ? 2 : 3, i == 5 ? 0 : 1); \
        if (FIN && ((i >= 2 && i <= 8 && !(i & 1)) || (i >= 11 && i <= 14))) { \
          const int c_ = i <= 8 ? 3 + (i >> 1) : i - 11; \
          _Pragma("unroll") for (int e = 0; e < 8; ++e) o[c_ >> 1][(c_ & 1) * 8 + e] *= alpha; \
          st128(hb + c_ * 1024, (c_ & 1) ? pack8<8>(o[c_ >> 1]) : pack8<0>(o[c_ >> 1])); \
        } \
        SBAR(); \
      } \
      ROT(); } while (0)
#define TICK_B(S0, S1, T, newq, NEWU, OZ, XC) do { \
      const unsigned vb_ = laneV + c0, kb_ = laneK + c1, dd_ = c0 + w * 4096; \
      const char* sp_ = Kc + kvoff(((T) + 3) >> 1); \
      _Pragma("unroll") for (int i = 0; i < 16; ++i) { \
        if (i == 0 || i == 4 || i == 8) { if (OZ && i == 0) WL(12); else WL(8); SBAR(); } \
        o[i & 3] = MFMA32(fr[i & 7], (i < 4 ? pk0 : i < 8 ? pk1 : i < 12 ? pk2 : pk3), (OZ && i < 4) ? f32x16{} : o[i & 3]); \
        if (i < 8) fr[i] = RDV(vb_, i + 8); \
        if (i == 10) { \
          SBAR(); WAITV(6); if (!(XC)) WAITV(4); WL(0); LBAR(T); \
          fr[0] = RDK(kb_, 0); fr[1] = RDK(kb_, 1); fr[2] = RDK(kb_, 2); \
        } \
        if (i > 10) fr[i - 8] = RDK(kb_, i - 8); \
        if (i == 11) KPIECE(0, dd_, sp_); if (i == 12) KPIECE(1, dd_, sp_); if (i == 13) KPIECE(2, dd_, sp_); if (i == 14) KPIECE(3, dd_, sp_); \
        if (i >= 1 && i <= 3) MAXG(S0, S1, i); \
        if (i == 4) { SBAR(); REFUPD(S0, S1, NEWU); } \
        if (i >= 5) { \
          constexpr int e0_[12] = {0, 1, 2, 3, 5, 7, 8, 9, 10, 11, 12, 14}; \
          _Pragma("unroll") for (int e = e0_[i - 5]; e < e0_[i - 4]; ++e) EXP1(S0, e); \
          PIN(l); } \
        if (i == 12) { pk0 = pack8<0>(S0); PIN(pk0); } \
        SBAR(); \
      } \
      if (newq) { \
        asm volatile("s_barrier" ::: "memory"); \
        _Pragma("unroll") for (int ks = 0; ks < 8; ++ks) qf[ks] = lds128(hb + ks * 1024); \
      } \
      ROT(); } while (0)
#pragma nounroll
    for (int P = 0; P < 4; ++P) {
      const int T0 = 16 * P;
      TICK_A(sa0, sa1, sb0, sb1, T0, true, false, P < 3, P + 1, 8, 4);
      TICK_B(sa0, sa1, T0 + 1, false, true, false, P < 3);
      TICK_A(sb0, sb1, sa0, sa1, T0 + 2, false, true, false, 0, 0, 0);
      {
        TICK_B(sb0, sb1, T0 + 3, false, false, true, false);
#pragma nounroll
        for (int k = 0; k < 3; ++k) {
          const int T = T0 + 4 + 4 * k;
          const int xu = P + (k == 2 ? 2 : 1);
          TICK_A(sa0, sa1, sb0, sb1, T, false, false, xu < 4, xu, k == 2 ? 1 : 4 + 2 * k, k == 2 ? 9 : 2 - 2 * k);
          TICK_B(sa0, sa1, T + 1, false, false, false, xu < 4);
          TICK_A(sb0, sb1, sa0, sa1, T + 2, false, false, P < 3 && k < 2, xu, 5 + 2 * k, 3 - 2 * k);
          TICK_B(sb0, sb1, T + 3, k == 2 && P < 3, false, false, P < 3 && k < 2);
        }
      }
    }
    {
      const unsigned vb_ = laneV + c1;
      WAITV(4); BAR(); BAR();
#define DI(i) ((((i) & 1) | (((i) & 8) >> 2) | (((i) & 6) << 1)) ^ 2)
#pragma unroll
      for (int i = 0; i < 8; ++i) fr[i] = RDV(vb_, DI(i));
      if (resc) {
#pragma unroll
        for (int e = 0; e < 64; ++e) o[e >> 4][e & 15] *= alpha;
      }
#pragma unroll
      for (int e = 14; e < 32; ++e) EXPE(sb0, sb1, e);
      pk1 = pack8<8>(sb0); pk2 = pack8<0>(sb1); pk3 = pack8<8>(sb1);
      auto r2_ = __builtin_amdgcn_permlane32_swap(F2U(l), F2U(l), false, false);
      alpha = __builtin_amdgcn_rcpf(U2F(r2_[0]) + U2F(r2_[1]));
      SBAR();
#pragma unroll
      for (int i = 0; i < 16; ++i) {
        o[DI(i) & 3] = MFMA32(fr[i & 7], ((i & 6) == 0 ? pk0 : (i & 6) == 2 ? pk1 : (i & 6) == 4 ? pk2 : pk3), o[DI(i) & 3]);
        if (i < 8) fr[i] = RDV(vb_, DI(i + 8));
        if (i >= 9 && (i & 1)) {
          const int c_ = 4 + ((i - 9) >> 1);
#pragma unroll
          for (int e = 0; e < 8; ++e) o[c_ >> 1][(c_ & 1) * 8 + e] *= alpha;
          st128(hb + c_ * 1024, (c_ & 1) ? pack8<8>(o[c_ >> 1]) : pack8<0>(o[c_ >> 1]));
        }
        SBAR();
      }
      BAR();
#pragma unroll
      for (int c_ = 0; c_ < 4; ++c_) {
#pragma unroll
        for (int e = 0; e < 8; ++e) o[c_ >> 1][(c_ & 1) * 8 + e] *= alpha;
        st128(hb + c_ * 1024, (c_ & 1) ? pack8<8>(o[c_ >> 1]) : pack8<0>(o[c_ >> 1]));
      }
      BAR();
    }
    MT(3); WAITV(0);
    STAMP_OUT();
  } else {
    const int j = wid - 4;
    f32x4 acc[9][4] = {};
    unsigned e0, e1, e2, e3;
    ASTAMP_DECL();
    {
    LANE_CONSTS();
    const char* WQ = (br ? wqB : wqA) + g * 2048 + j * 128;
    const char* WO = (br ? woB : woA) + (size_t)g * 1024 * 1152;
    const unsigned ring = L_W + j * RG;
    const unsigned voffQ = (lane >> 3) * 8192 + (((lane & 7) ^ ((g4 & 1) << 2)) * 16);
    const unsigned dvo = voffQ ^ ((lane >> 3) * 1152 + j * 256 + (((lane & 7) ^ ((g4 & 1) << 1)) * 16));
    const unsigned pos = lane >> 1;
    const unsigned voffC = (pos ^ (((pos >> 3) & 1) << 2)) * 1152 + 1024 + j * 32 + (lane & 1) * 16;
    const int dOB = (g >> 1) ? -(int)RG : (int)RG;
    f32x16 aq[2][2];
    auto isO = [](int n) -> int { return (int)(n >= 52) | (int)((unsigned)(n - 22) < 8u) | (int)((unsigned)(n - 39) < 8u); };
    auto woff = [&](int n) -> unsigned {
      const int qi = n - (n >= 30 ? 8 : 0) - (n >= 47 ? 8 : 0), u = qi / 9, s = qi - 9 * u;
      const int oi = n >= 52 ? n - 36 : n >= 39 ? n - 31 : n - 22;
      return isO(n) ? (unsigned)(((((oi >> 3) + rot) & 3) * 256 + (oi & 1) * 128 + ((oi & 6) ^ 4) * 16) * 9) | 65536u
                    : (unsigned)(xblk(r0, u, s) * 4096 + ((u + rot) & 3) * 4);
    };
    auto build = [&](int base) -> unsigned {
      const int n = base + lane_id(), m = n + 1;
      const int qi = m - (m >= 30 ? 8 : 0) - (m >= 47 ? 8 : 0), u = qi / 9, s = (qi - 9 * u) & 15;
      const int oi = m >= 52 ? m - 36 : m >= 39 ? m - 31 : m - 22;
      const unsigned boff = isO(m) ? (unsigned)((((oi & 7) >> 1) ^ 2) + (oi & 1) * 28) : (unsigned)((u == 0 ? 0x421032104ull : 0x410321094ull) >> (4 * s)) & 15u;
      const unsigned qw = n == 8 || n == 17 || n == 34 || n == 51;
      const unsigned nba = !(n == 8 || n == 11 || n == 20 || n == 37 || n >= 61);
      const unsigned last = qw || n == 21 || n == 29 || n == 38 || n == 46 || n == 67;
      return woff(n + 2 < 67 ? n + 2 : 67) | boff << 27 | nba << 21 | (unsigned)(n == 12 || n == 59) << 22 | last << 23 | qw << 24 | (unsigned)(n == 51) << 25 | (unsigned)isO(m) << 26;
    };
    u32x4 rs = {0u, 0u, 0x7fffffffu, 0x00020000u};
    unsigned soff, gstep, pstep, ve, vo; unsigned long long cmask;
    auto src = [&](unsigned D) {
      const bool io = (D & 65536u) != 0;
      const unsigned long long bp = (unsigned long long)(io ? WO : WQ);
      rs[0] = (unsigned)bp; rs[1] = (unsigned)(bp >> 32);
      soff = (D & 65535u) << 7; gstep = io ? 128u : 32u * 8192u; pstep = io ? 9216u - 1024u : 65536u - 1024u;
      cmask = io ? ~0ull : 0ull;
      const unsigned msk = io ? ~0u : 0u;
      ve = voffQ ^ (dvo & msk); vo = ve ^ (64u & msk);
      PINS(rs); PINS(soff); PINS(gstep); PINS(pstep); PINS(cmask); PIN(ve); PIN(vo);
    };
#define RF(G, c, half) do { if ((c) == 0) SETM0(ring + (half) + (G) * 4096); bdma<(c) * 1024>(rs, soff + (G) * gstep + (c) * pstep, ((c) & 1) ? vo : ve); } while (0)
#define RFC(half) asm volatile("s_mov_b32 m0, %0\n\ts_mov_b64 exec, %1\n\tbuffer_load_dwordx4 %2, %3, %4 offen lds\n\ts_mov_b64 exec, -1" ::"s"(ring + (half) + 8192), "s"(cmask), "v"(voffC), "s"(rs), "s"(soff) : "memory")
    unsigned ev;
    asm volatile("global_load_dword %0, %1, %2 sc1" : "=v"(ev) : "v"((bid ^ (lane & 3)) * 256), "s"(ws + WS_FLAG) : "memory");
    src(woff(0)); RF(0, 0, 0); RF(0, 1, 0); RF(0, 2, 0); RF(0, 3, 0); RF(1, 0, 0); RF(1, 1, 0); RF(1, 2, 0); RF(1, 3, 0);
    ASTAMP_T(1);
    unsigned tab = build(0);
    PIN(tab); ASTAMP_T(2);
    src(woff(1)); RF(0, 0, 9216); RF(0, 1, 9216); RF(0, 2, 9216); RF(0, 3, 9216);
    {
      GAS unsigned* fl = (GAS unsigned*)(ws + WS_FLAG);
      asm volatile("s_waitcnt vmcnt(12)\n\tv_add_u32 %0, 1, %0" : "+v"(ev)::"memory");
      e0 = __builtin_amdgcn_readlane(ev, 0); e1 = __builtin_amdgcn_readlane(ev, 1); e2 = __builtin_amdgcn_readlane(ev, 2); e3 = __builtin_amdgcn_readlane(ev, 3);
      ABAR();
      if (j == 0 && lane >= 1 && lane < 4) __hip_atomic_store(fl + (bid ^ lane) * 64 + 32 + lane, ev, __ATOMIC_RELAXED, __HIP_MEMORY_SCOPE_AGENT);
    }
    __builtin_amdgcn_s_setprio(2);
    unsigned half = 0, D = 4u << 27 | 1u << 24;
    int ti = 0;
#define XBAR() asm volatile("s_bitcmp0_b32 %0, 22\n\ts_cbranch_scc1 1f\n\ts_barrier\n\ts_barrier\n\ts_barrier\n\ts_barrier\n\ts_barrier\n\ts_barrier\n\ts_barrier\n\ts_barrier\n1:\n\t" \
      "s_bitcmp0_b32 %0, 21\n\ts_cbranch_scc1 2f\n\ts_barrier\n2:" ::"s"(D) : "memory", "scc")
#pragma nounroll
    for (int run = 0; run < 3; ++run) {
      {
#define QA(ks, d) tr2((aa ^ ((d) << 6)) + (ks) * 2048, (aa ^ ((d) << 6)) + (ks) * 2048 + 512)
#define QB(ks, q) lds128((bb ^ ((ks) << 5)) + (q) * 2 * RG)
#define QBB() ((laneQB & 1024u) * ((D >> 27) == 9u ? 8u : 0u) + laneQB + ((D >> 27) << 11))
#define QM1(d, q, fa, fb) do { aq[d][q] = MFMA32(fa, fb, aq[d][q]); SBAR(); } while (0)
        LANE_CONSTS();
        unsigned laneQB = L_H + (r >> 4) * RG + (r & 15) * 128 + ((h ^ ((r >> 1) & 7)) * 16);
        unsigned aa = ring + h * 1024 + q4 * 128 + (((q4 >> 1) & 1) * 64) + gi * 32 + pp * 8 + half, bb = QBB();
        bf16x8 r0_, r1, r2, x0, x1, y0, y1;
        PIN(laneQB); PIN(aa); PIN(bb); SBAR();
        WAITV(8);
        r0_ = QA(0, 0); x0 = QB(0, 0); x1 = QB(0, 1); SBAR(); r1 = QA(0, 1); SBAR(); r2 = QA(1, 0); SBAR(); y0 = QB(1, 0); y1 = QB(1, 1); SBAR();
#define QZ1(d, q, fa, fb, Z) do { aq[d][q] = MFMA32(fa, fb, Z ? f32x16{} : aq[d][q]); SBAR(); } while (0)
#define QBODY(Z) do { \
          const unsigned oh = half ^ 9216u; \
          QZ1(0, 0, r0_, x0, Z); RFC(oh); QZ1(0, 1, r0_, x1, Z); r0_ = QA(1, 1); RF(1, 0, oh); SBAR(); \
          QZ1(1, 0, r1, x0, Z); QZ1(1, 1, r1, x1, Z); RF(1, 1, oh); WAITV(6); r1 = QA(2, 0); x0 = QB(2, 0); x1 = QB(2, 1); SBAR(); \
          QM1(0, 0, r2, y0); RF(1, 2, oh); QM1(0, 1, r2, y1); r2 = QA(3, 0); SBAR(); \
          QM1(1, 0, r0_, y0); RF(1, 3, oh); QM1(1, 1, r0_, y1); r0_ = QA(2, 1); y0 = QB(3, 0); y1 = QB(3, 1); \
          D = __builtin_amdgcn_readlane(tab, ti); ++ti; src(D); SBAR(); \
          QM1(0, 0, r1, x0); RF(0, 0, half); QM1(0, 1, r1, x1); r1 = QA(3, 1); SBAR(); \
          aa += 9216u - 2 * half; PIN(laneQB); bb = QBB(); PIN(aa); PIN(bb); \
          QM1(1, 0, r0_, x0); RF(0, 1, half); QM1(1, 1, r0_, x1); \
          WL(0); WAITL0(); XBAR(); WAITV(6); \
          r0_ = QA(0, 0); x0 = QB(0, 0); x1 = QB(0, 1); SBAR(); \
          QM1(1, 0, r1, y0); RF(0, 2, half); QM1(1, 1, r1, y1); r1 = QA(0, 1); SBAR(); \
          QM1(0, 0, r2, y0); RF(0, 3, half); QM1(0, 1, r2, y1); r2 = QA(1, 0); y0 = QB(1, 0); y1 = QB(1, 1); SBAR(); \
          half ^= 9216u; } while (0)
#pragma nounroll
        do {
          if (D & (1u << 24)) QBODY(true);
#pragma nounroll
          do { QBODY(false); } while (!(D & (1u << 23)));
          if (D & (1u << 24)) {
            const unsigned d = L_H + (j >> 1) * 2 * RG + (j & 1) * 4096 + lane_id() * 16;
#pragma unroll
            for (int d2 = 1; d2 >= 0; --d2)
#pragma unroll
              for (int qt = 0; qt < 2; ++qt) {
                aq[d2][qt] *= SL2E;
                st128(d + qt * RG + d2 * 2048, pack8<0>(aq[d2][qt]));
                st128(d + qt * RG + d2 * 2048 + 1024, pack8<8>(aq[d2][qt]));
              }
            ABAR();
            y0 = QB(1, 0); y1 = QB(1, 1); SBAR();
          }
        } while (!(D & (1u << 26)));
        if (D & (1u << 25)) {
          const int l = lane_id(), o2 = (l < 13 ? l : 13) + 18, o1 = l + 17;
          tab = (unsigned)(((((o2 >> 3) + rot) & 3) * 256 + (o2 & 1) * 128 + ((o2 & 6) ^ 4) * 16) * 9) | 65536u | (unsigned)((((o1 & 7) >> 1) ^ 2) + (o1 & 1) * 28) << 27
                | (unsigned)(l <= 8) << 21 | (unsigned)(l == 7) << 22 | (unsigned)(l == 15) << 23;
          ti = 0; ABAR(); ABAR(); ABAR();
        }
      }
      {
#define OA(t) tr2((aa ^ (((t) & 3) << 5)) + ((t) >> 2) * 4096, (aa ^ (((t) & 3) << 5)) + ((t) >> 2) * 4096 + 512)
#define OM1(t, f, q) acc[t][q] = MFMA16(f, bq[q], acc[t][q])
#define OM2(t, f, q) do { OM1(t, f, q); OM1(t, f, q + 1); SBAR(); } while (0)
#define OM(t, f) do { OM2(t, f, 0); OM2(t, f, 2); } while (0)
        LANE_CONSTS();
        const unsigned laneOC = ring + 8192 + (8 * g4 + 4 * (g4 & 1) + q4) * 32 + p4 * 8;
        const unsigned laneOB = L_H + (g >> 1) * RG + (g4 >> 1) * 1024 + (g4 & 1) * 512 + (g & 1) * 256 + i16 * 16;
        unsigned aa = ring + g4 * 1024 + q4 * 128 + ((((q4 >> 1) & 1) | ((g4 & 1) << 1)) * 32) + p4 * 8 + half;
        unsigned ba = laneOB + ((D >> 27) << 11), bb = ba + dOB;
        bf16x8 bq[4], f0, f1, f2;
        WAITV(8);
        f0 = OA(0); bq[0] = lds128(ba); bq[2] = lds128(bb); bq[1] = lds128(ba ^ 256); f1 = OA(1); bq[3] = lds128(bb ^ 256); f2 = OA(2); SBAR();
#pragma nounroll
        do {
          const unsigned oh = half ^ 9216u;
          OM1(0, f0, 0); SBAR(); RFC(oh); OM1(0, f0, 2); OM1(0, f0, 1); SBAR(); RF(1, 0, oh);
          OM1(1, f1, 0); OM1(1, f1, 2); OM1(1, f1, 1); OM1(0, f0, 3); SBAR(); f0 = OA(3); RF(1, 1, oh); SBAR();
          OM1(1, f1, 3); SBAR(); WAITV(6); f1 = OA(4); SBAR();
          OM2(2, f2, 0); RF(1, 2, oh); OM2(2, f2, 2); f2 = OA(5); SBAR();
          OM2(3, f0, 0); RF(1, 3, oh); OM2(3, f0, 2); f0 = OA(6); D = __builtin_amdgcn_readlane(tab, ti); ++ti; src(D); SBAR();
          OM2(4, f1, 0); RF(0, 0, half); OM2(4, f1, 2); f1 = OA(7); SBAR();
          OM2(5, f2, 0); RF(0, 1, half); OM2(5, f2, 2); { const unsigned ac = laneOC + half; f2 = tr2(ac, ac ^ 128); } SBAR();
          aa += 9216u - 2 * half; ba = laneOB + ((D >> 27) << 11); bb = ba + dOB; PIN(aa); PIN(ba); PIN(bb);
          OM2(6, f0, 0); RF(0, 2, half); OM2(6, f0, 2);
          OM1(7, f1, 0); OM1(7, f1, 2); OM1(8, f2, 0); OM1(8, f2, 2); SBAR();
          WL(0); WAITL0(); XBAR(); WAITV(7);
          f0 = OA(0); bq[0] = lds128(ba); bq[2] = lds128(bb); SBAR();
          OM1(7, f1, 1); SBAR(); RF(0, 3, half); OM1(8, f2, 1); SBAR(); bq[1] = lds128(ba ^ 256); SBAR();
          OM1(7, f1, 3); SBAR(); f1 = OA(1); OM1(8, f2, 3); SBAR(); bq[3] = lds128(bb ^ 256); f2 = OA(2); SBAR();
          half ^= 9216u;
        } while (!(D & (1u << 23)));
      }
    }
    }
    LANE_CONSTS();
    ASTAMP_T(3);
    const __amdgpu_buffer_rsrc_t rs = __builtin_amdgcn_make_buffer_rsrc((void*)(ws + WS_SLAB), 0, (int)WS_SLAB_BYTES, 0x00020000);
#pragma unroll
    for (int i = 1; i < 4; ++i) {
      const int so = (((bid ^ i) * 3 + (i - 1)) * 4 + j) * 4608;
#pragma unroll
      for (int t = 0; t < 8; t += 2) {
        const u32x4 v = {cvtpk(acc[t][i][0], acc[t][i][1]), cvtpk(acc[t][i][2], acc[t][i][3]), cvtpk(acc[t + 1][i][0], acc[t + 1][i][1]), cvtpk(acc[t + 1][i][2], acc[t + 1][i][3])};
        __builtin_amdgcn_raw_buffer_store_b128(v, rs, lane * 16 + t * 512, so, 16);
      }
      const u32x2 v = {cvtpk(acc[8][i][0], acc[8][i][1]), cvtpk(acc[8][i][2], acc[8][i][3])};
      __builtin_amdgcn_raw_buffer_store_b64(v, rs, lane * 8 + 4096, so, 16);
    }
    GAS unsigned* fl = (GAS unsigned*)(ws + WS_FLAG);
    const unsigned want = (lane & 3) == 0 ? e0 : (lane & 3) == 1 ? e1 : (lane & 3) == 2 ? e2 : e3;
    if (j == 0) {
      for (unsigned spins = 0; spins < (1u << 20); ++spins) {
        unsigned v = e0;
        if (lane >= 1 && lane < 4) v = __hip_atomic_load(fl + bid * 64 + 32 + lane, __ATOMIC_RELAXED, __HIP_MEMORY_SCOPE_AGENT);
        if (ALL(v == e0)) break;
        __builtin_amdgcn_s_sleep(1);
      }
    }
    WAITV(0);
    BAR();
    ASTAMP_T(4);
    if (j == 0 && lane == 0) __hip_atomic_store(fl + bid * 64, e0, __ATOMIC_RELAXED, __HIP_MEMORY_SCOPE_AGENT);
    for (unsigned spins = 0; spins < (1u << 20); ++spins) {
      unsigned v = want;
      if (lane >= 1 && lane < 4) v = __hip_atomic_load(fl + (bid ^ lane) * 64, __ATOMIC_RELAXED, __HIP_MEMORY_SCOPE_AGENT);
      if (ALL(v == want)) break;
      __builtin_amdgcn_s_sleep(1);
    }
    __builtin_amdgcn_fence(__ATOMIC_ACQUIRE, "wavefront");
    ASTAMP_T(5);
    u32x4 pv[3][4]; u32x2 pw[3];
#pragma unroll
    for (int i = 1; i < 4; ++i) {
      const int so = ((bid * 3 + (i - 1)) * 4 + j) * 4608;
#pragma unroll
      for (int t = 0; t < 4; ++t) pv[i - 1][t] = __builtin_amdgcn_raw_buffer_load_b128(rs, lane * 16 + t * 1024, so, 16);
      pw[i - 1] = __builtin_amdgcn_raw_buffer_load_b64(rs, lane * 8 + 4096, so, 16);
    }
    char* orow = out + ((size_t)(b * 64 + 16 * g + i16) * 1152 + br * 576) * 2;
    unsigned c0_ = 0x00003f80u, c1_ = 0x3f800000u; PIN(c0_); PIN(c1_);
    const bf16x2 sel0 = __builtin_bit_cast(bf16x2, c0_), sel1 = __builtin_bit_cast(bf16x2, c1_);
    u32x2 pk[9];
#pragma unroll
    for (int t = 0; t < 9; ++t) {
      f32x4 v = acc[t][0];
#pragma unroll
      for (int i = 0; i < 3; ++i) {
        const bf16x2 lo = __builtin_bit_cast(bf16x2, t < 8 ? pv[i][t >> 1][(t & 1) * 2] : pw[i][0]), hi = __builtin_bit_cast(bf16x2, t < 8 ? pv[i][t >> 1][(t & 1) * 2 + 1] : pw[i][1]);
        v[0] = __builtin_amdgcn_fdot2_f32_bf16(lo, sel0, v[0], false); v[1] = __builtin_amdgcn_fdot2_f32_bf16(lo, sel1, v[1], false);
        v[2] = __builtin_amdgcn_fdot2_f32_bf16(hi, sel0, v[2], false); v[3] = __builtin_amdgcn_fdot2_f32_bf16(hi, sel1, v[3], false);
      }
      pk[t] = (u32x2){cvtpk(v[0], v[1]), cvtpk(v[2], v[3])};
      if (t & 1) {
        auto s0 = __builtin_amdgcn_permlane16_swap(pk[t - 1][0], pk[t][0], false, false);
        auto s1 = __builtin_amdgcn_permlane16_swap(pk[t - 1][1], pk[t][1], false, false);
        *(GAS u32x4*)(orow + (128 * j + 16 * (t - 1) + 16 * (g4 & 1) + 8 * (g4 >> 1)) * 2) = (u32x4){s0[0], s1[0], s0[1], s1[1]};
      }
    }
    *(GAS u32x2*)(orow + (512 + 16 * j + 4 * g4) * 2) = pk[8];
    ASTAMP_OUT();
  }
}

#ifdef __HIP_DEVICE_COMPILE__
#pragma clang attribute pop
#endif
extern "C" void kernel_launch(void* const* d_in, const int* in_sizes, int n_in, void* d_out, int out_size, void* d_ws, size_t ws_size,
                              hipStream_t stream) {
  (void)in_sizes; (void)n_in; (void)out_size; (void)ws_size;
  (void)hipFuncSetAttribute((const void*)fused_gqa, hipFuncAttributeMaxDynamicSharedMemorySize, (int)L_TOT);
  hipLaunchKernelGGL(fused_gqa, dim3(256), dim3(512), L_TOT, stream, (const char*)d_in[0], (const char*)d_in[1], (const char*)d_in[2],
                     (const char*)d_in[3], (const char*)d_in[4], (const char*)d_in[5], (const char*)d_in[6], (const char*)d_in[7],
                     (const char*)d_in[8], (const char*)d_in[9], (char*)d_out, (char*)d_ws);
}
```

```cpp
#include <hip/hip_runtime.h>
#include <stdint.h>
#ifdef __HIP_DEVICE_COMPILE__
#pragma clang attribute push(__attribute__((target("no-packed-fp32-ops"))), apply_to = function)
#endif
#define U2F(x) __builtin_bit_cast(float, (unsigned)(x))
#define F2U(x) __builtin_bit_cast(unsigned, (float)(x))
#define ALL(c) (__builtin_amdgcn_ballot_w64(!(c)) == 0)

typedef __attribute__((ext_vector_type(4))) short s16x4;
typedef __attribute__((ext_vector_type(8))) short bf16x8;
typedef __attribute__((ext_vector_type(4))) float f32x4;
typedef __attribute__((ext_vector_type(16))) float f32x16;
typedef __attribute__((ext_vector_type(4))) unsigned u32x4;
typedef __attribute__((ext_vector_type(2))) unsigned u32x2;
#define LAS __attribute__((address_space(3)))
#define GAS __attribute__((address_space(1)))
#define DEVI __device__ __forceinline__

constexpr unsigned L_H = 0;
constexpr unsigned RG = 28672;
constexpr unsigned L_W = 10240;
constexpr unsigned L_KV = 114688;
constexpr unsigned L_TOT = 163840;
constexpr size_t WS_SLAB = 0;
constexpr size_t WS_SLAB_BYTES = (size_t)256 * 3 * 4 * 4608;
constexpr size_t WS_FLAG = (size_t)32 << 20;

constexpr float SL2E = 0.088388347648318447f * 1.4426950408889634f;

DEVI bf16x8 lds128(unsigned a) { return *(LAS bf16x8*)(a); }
DEVI void st128(unsigned a, bf16x8 v) { *(LAS bf16x8*)(a) = v; }
DEVI bf16x8 tr2(unsigned a0, unsigned a1) {
  s16x4 lo = __builtin_amdgcn_ds_read_tr16_b64_v4i16((LAS s16x4*)(a0));
  s16x4 hi = __builtin_amdgcn_ds_read_tr16_b64_v4i16((LAS s16x4*)(a1));
  return (bf16x8){lo[0], lo[1], lo[2], lo[3], hi[0], hi[1], hi[2], hi[3]};
}
typedef __attribute__((ext_vector_type(2))) float f32x2;
typedef __attribute__((ext_vector_type(2))) __bf16 bf16x2;
DEVI unsigned cvtpk(float lo, float hi) { return __builtin_bit_cast(unsigned, __builtin_convertvector((f32x2){lo, hi}, bf16x2)); }
#define PIN(x) asm volatile("" : "+v"(x))
template <int B> DEVI bf16x8 pack8(const f32x16& v) {
  u32x4 w = {cvtpk(v[B], v[B + 1]), cvtpk(v[B + 2], v[B + 3]), cvtpk(v[B + 4], v[B + 5]), cvtpk(v[B + 6], v[B + 7])};
  return __builtin_bit_cast(bf16x8, w);
}
template <int OFF> DEVI void dma_nt2(const char* sb, unsigned voff) {
  asm volatile("global_load_lds_dwordx4 %0, %1 offset:%2 nt" ::"v"(voff), "s"(sb), "i"(OFF) : "memory");
}
template <int OFF> DEVI void dma_nt(unsigned lds, const char* sb, unsigned voff) {
  asm volatile("s_mov_b32 m0, %0\n\ts_nop 0\n\tglobal_load_lds_dwordx4 %1, %2 offset:%3 nt" ::"s"(lds), "v"(voff), "s"(sb), "i"(OFF) : "memory");
}
DEVI void dma(unsigned lds, const char* sb, unsigned voff) {
  asm volatile("s_mov_b32 m0, %0\n\ts_nop 0\n\tglobal_load_lds_dwordx4 %1, %2" ::"s"(lds), "v"(voff), "s"(sb) : "memory");
}
template <int OFF> DEVI void bdma(u32x4 rs, unsigned soff, unsigned voff) {
  asm volatile("buffer_load_dwordx4 %0, %1, %2 offen offset:%3 lds" ::"v"(voff), "s"(rs), "s"(soff), "i"(OFF) : "memory");
}
#define SETM0(x) asm volatile("s_mov_b32 m0, %0\n\ts_nop 0" ::"s"(x) : "memory")
#define PINS(x) asm volatile("" : "+s"(x))
#define WAITV(n) asm volatile("s_waitcnt vmcnt(" #n ")" ::: "memory")
#define WAITL0() asm volatile("s_waitcnt lgkmcnt(0)" ::: "memory")
#define BAR() asm volatile("s_waitcnt lgkmcnt(0)\n\ts_barrier" ::: "memory")
#define SBAR() __builtin_amdgcn_sched_barrier(0)
#define WL(n) __builtin_amdgcn_s_waitcnt(0xC07F | ((n) << 8))
#define MFMA32(a, b, c) __builtin_amdgcn_mfma_f32_32x32x16_bf16(a, b, c, 0, 0, 0)
#define MFMA16(a, b, c) __builtin_amdgcn_mfma_f32_16x16x32_bf16(a, b, c, 0, 0, 0)

DEVI int lane_id() { int l; asm volatile("v_mbcnt_lo_u32_b32 %0, -1, 0\n\tv_mbcnt_hi_u32_b32 %0, -1, %0" : "=v"(l)); return l; }
#define STAMP_DECL()
#define MT(i)
#define MBAR() BAR()
#define LBAR(T) BAR()
#define STAMP_OUT()
#define ASTAMP_DECL()
#define ASTAMP_T(i)
#define ABAR() BAR()
#define AWAITV(n) WAITV(n)
#define ASTAMP_OUT()
DEVI int xblk(int r0, int unit, int s) { int v = r0 + 9 - unit + s; v -= (v >= 18) ? 18 : 0; v -= (v >= 9) ? 9 : 0; return v; }

__global__ __launch_bounds__(512, 2) void fused_gqa(const char* __restrict__ xA, const char* __restrict__ kA, const char* __restrict__ vA,
                                                    const char* __restrict__ wqA, const char* __restrict__ woA, const char* __restrict__ xB,
                                                    const char* __restrict__ kB, const char* __restrict__ vB, const char* __restrict__ wqB,
                                                    const char* __restrict__ woB, char* __restrict__ out, char* __restrict__ ws) {
  extern __shared__ __attribute__((aligned(16))) char shm[];
  const int bid = (int)__builtin_amdgcn_workgroup_id_x(), br = (bid >> 2) & 1, g = bid & 3, b = bid >> 3;
  const int wid = __builtin_amdgcn_readfirstlane((int)__builtin_amdgcn_workitem_id_x() >> 6);
  const int rot = b & 3, r0 = b >> 2;
#define LANE_CONSTS() const int lane = lane_id(); \
  const int r = lane & 31, h = lane >> 5, i16 = lane & 15, q4 = i16 >> 2, p4 = i16 & 3, pp = ((p4 & 1) << 1) | (p4 >> 1), gi = (lane >> 4) & 1, g4 = lane >> 4; \
  (void)r; (void)h; (void)q4; (void)p4; (void)pp; (void)gi; (void)g4;

  if (wid < 4) {
    const int w = wid;
    LANE_CONSTS();
    STAMP_DECL();
    const char* X = (br ? xB : xA) + (size_t)(b * 64 + 16 * w) * 1152;
    const char* Kc = (br ? kB : kA) + (size_t)(b * 16 + 4 * g) * 131072 + w * 4096;
    const char* Vc = (br ? vB : vA) + (size_t)(b * 16 + 4 * g) * 131072 + w * 4096;
    const unsigned laneK = r * 256 + ((h ^ (r & 15)) * 16);
    const unsigned laneV = (4 * h + q4) * 256 + q4 * 64 + gi * 32 + pp * 8;
    const unsigned voffK = g4 * 256 + ((i16 ^ g4) * 16);
    const unsigned voffV = g4 * 256 + ((i16 ^ (g4 << 2)) * 16);
    const unsigned voffX = (lane >> 3) * 1152 + (((lane & 7) ^ g4) * 16);
    const unsigned hb = L_H + w * RG + lane * 16;
    auto kvoff = [&](int idx) -> unsigned { idx = idx < 31 ? idx : 31; return (unsigned)((idx + 8 * rot) & 31) * 16384u; };
    auto issueX = [&](int unit, int s, int slot) {
      const char* p = X + xblk(r0, unit, s) * 128;
      const unsigned d = L_H + w * RG + slot * 2048;
      dma(d, p, voffX); dma(d + (slot == 9 ? 9216 : 1024), p + 9216, voffX ^ 64);
    };
#define KPIECE(c, d, s) do { if ((c) == 0) dma_nt<0>(d, s, voffK); else dma_nt2<(c) * 1024>(s, voffK ^ ((c) * 64)); } while (0)
#define VPIECE(c, d, s) do { if ((c) == 0) dma_nt<0>(d, s, voffV); else dma_nt2<(c) * 1024>(s, voffV); } while (0)
#define RDK(kb, i) lds128(((kb) ^ (((i) >> 1) << 5)) + ((i) & 1) * 8192)
#define RDV(vb, i) tr2(((vb) ^ (((i) & 3) << 6)) + ((i) >> 2) * 4096, ((vb) ^ (((i) & 3) << 6)) + ((i) >> 2) * 4096 + 2048)
    bf16x8 qf[8], fr[8], pk0 = {}, pk1 = {}, pk2, pk3;
    f32x16 o[4] = {}, sa0, sa1, sb0 = {}, sb1 = {}, nmv = {};
    float l = 1.f, alpha = 1.f, mx = 0.f;
    bool resc = false;
#define EXP1(S, i) do { float t_ = S[i]; PIN(t_); t_ = __builtin_amdgcn_exp2f(t_); l += t_; S[i] = t_; } while (0)
#define EXPE(S0, S1, e) do { if ((e) < 16) EXP1(S0, (e) & 15); else EXP1(S1, (e) & 15); } while (0)
#define MAXG(S0, S1, gq) do { \
      if ((gq) == 1) { mx = __builtin_fmaxf(__builtin_fmaxf(S0[0], S0[1]), S0[2]); _Pragma("unroll") for (int i_ = 3; i_ < 13; i_ += 2) mx = __builtin_fmaxf(__builtin_fmaxf(mx, S0[i_]), S0[i_ + 1]); } \
      if ((gq) == 2) { mx = __builtin_fmaxf(__builtin_fmaxf(mx, S0[13]), S0[14]); mx = __builtin_fmaxf(__builtin_fmaxf(mx, S0[15]), S1[0]); _Pragma("unroll") for (int i_ = 1; i_ < 7; i_ += 2) mx = __builtin_fmaxf(__builtin_fmaxf(mx, S1[i_]), S1[i_ + 1]); } \
      if ((gq) == 3) { _Pragma("unroll") for (int i_ = 7; i_ < 15; i_ += 2) mx = __builtin_fmaxf(__builtin_fmaxf(mx, S1[i_]), S1[i_ + 1]); mx = __builtin_fmaxf(mx, S1[15]); } \
      PIN(mx); } while (0)
#define REFUPD(S0, S1, NEWU) do { \
      if (NEWU || __builtin_amdgcn_ballot_w64(mx > 8.f) != 0) { \
        auto rr_ = __builtin_amdgcn_permlane32_swap(F2U(mx), F2U(mx), false, false); \
        float d_ = __builtin_fmaxf(U2F(rr_[0]), U2F(rr_[1])); \
        if (NEWU) { \
          auto r2_ = __builtin_amdgcn_permlane32_swap(F2U(l), F2U(l), false, false); \
          alpha = __builtin_amdgcn_rcpf(U2F(r2_[0]) + U2F(r2_[1])); l = 0.f; \
        } else { d_ = __builtin_fmaxf(d_, 0.f); alpha = __builtin_amdgcn_exp2f(-d_); l *= alpha; resc = true; } \
        const float n_ = (NEWU ? 0.f : nmv[0]) - d_; \
        _Pragma("unroll") for (int i_ = 0; i_ < 16; ++i_) { S0[i_] -= d_; S1[i_] -= d_; nmv[i_] = n_; } \
      } } while (0)
    issueX(0, 0, 4); issueX(0, 1, 0);
    MT(0);
#pragma unroll
    for (int i = 0; i < 4; ++i) st128(L_KV + 16384 + w * 4096 + i * 1024 + lane * 16, bf16x8{});
    WAITV(2); MBAR(); MT(1);
    issueX(0, 2, 1); issueX(0, 3, 2); issueX(0, 4, 3);
    WAITV(6); MBAR();
    issueX(0, 8, 4); WAITV(6); MBAR();
    issueX(0, 5, 0); WAITV(6); MBAR();
    issueX(0, 6, 1); WAITV(6); MBAR();
    issueX(0, 7, 2);
    {
      const unsigned d = L_KV + w * 4096;
      const char* s = Kc + kvoff(0);
      KPIECE(0, d, s); KPIECE(1, d, s); KPIECE(2, d, s); KPIECE(3, d, s);
    }
    issueX(1, 1, 9);
    WAITV(10); MBAR();
    WAITV(8); MBAR();
    WAITV(6); MBAR();
    MBAR();
    WAITV(2);
    MBAR();
    MT(2);
    {
#pragma unroll
      for (int ks = 0; ks < 8; ++ks) qf[ks] = lds128(hb + ks * 1024);
      const char* s = Kc + kvoff(1);
      const unsigned d = L_KV + w * 4096 + 32768;
      KPIECE(0, d, s); KPIECE(1, d, s); KPIECE(2, d, s); KPIECE(3, d, s);
      const unsigned kb = laneK + L_KV;
#pragma unroll
      for (int i = 0; i < 8; ++i) fr[i] = RDK(kb, i);
    }
    unsigned c0 = L_KV, c1 = L_KV + 16384, c2 = L_KV + 32768;
#define ROT() do { const unsigned t_ = c0; c0 = c1; c1 = c2; c2 = t_; } while (0)
#define TICK_A(D0, D1, E0, E1, T, CZ, FIN, XC, XU, XS, XSLOT) do { \
      const unsigned kb_ = laneK + c0, vb_ = laneV + c1, dd_ = c0 + w * 4096; \
      const char* sp_ = Vc + kvoff((T) >> 1); \
      if (!(FIN) && resc) { \
        _Pragma("unroll") for (int e = 0; e < 64; ++e) o[e >> 4][e & 15] *= alpha; \
        resc = false; \
      } \
      SBAR(); \
      _Pragma("unroll") for (int i = 0; i < 16; ++i) { \
        if (i == 0 || i == 4 || i == 8) { if (FIN && i == 4) WL(5); else if (FIN && i == 8) WL(6); else WL(4); SBAR(); } \
        if (i & 1) D1 = MFMA32(fr[i & 7], qf[i >> 1], i < 2 ? (CZ ? f32x16{} : nmv) : D1); else D0 = MFMA32(fr[i & 7], qf[i >> 1], i < 2 ? (CZ ? f32x16{} : nmv) : D0); \
        if (i < 8) fr[i] = RDK(kb_, i + 8); \
        if (i == 10) { \
          SBAR(); if (CZ) { WAITV(8); if (!(XC)) WAITV(4); } else WAITV(4); WL(0); LBAR(T); \
          fr[0] = RDV(vb_, 0); fr[1] = RDV(vb_, 1); fr[2] = RDV(vb_, 2); \
        } \
        if (i > 10) fr[i - 8] = RDV(vb_, i - 8); \
        if (i == 11) VPIECE(0, dd_, sp_); if (i == 12) VPIECE(1, dd_, sp_); if (i == 13) VPIECE(2, dd_, sp_); if (i == 14) VPIECE(3, dd_, sp_); \
        if (i < 9) { EXPE(E0, E1, 14 + 2 * i); EXPE(E0, E1, 15 + 2 * i); PIN(l); } \
        if (i == 1) { pk1 = pack8<8>(E0); PIN(pk1); } \
        if (i == 5) { pk2 = pack8<0>(E1); PIN(pk2); } \
        if (i == 9) { pk3 = pack8<8>(E1); PIN(pk3); } \
        if (i == 15 && (XC)) issueX(XU, XS, XSLOT); \
        if (CZ && (i == 5 || i == 7) && (XC)) issueX(XU, i == 5 ? 2 : 3, i == 5 ? 0 : 1); \
        if (FIN && ((i >= 2 && i <= 8 && !(i & 1)) || (i >= 11 && i <= 14))) { \
          const int c_ = i <= 8 ? 3 + (i >> 1) : i - 11; \
          _Pragma("unroll") for (int e = 0; e < 8; ++e) o[c_ >> 1][(c_ & 1) * 8 + e] *= alpha; \
          st128(hb + c_ * 1024, (c_ & 1) ? pack8<8>(o[c_ >> 1]) : pack8<0>(o[c_ >> 1])); \
        } \
        SBAR(); \
      } \
      ROT(); } while (0)
#define TICK_B(S0, S1, T, newq, NEWU, OZ, XC) do { \
      const unsigned vb_ = laneV + c0, kb_ = laneK + c1, dd_ = c0 + w * 4096; \
      const char* sp_ = Kc + kvoff(((T) + 3) >> 1); \
      _Pragma("unroll") for (int i = 0; i < 16; ++i) { \
        if (i == 0 || i == 4 || i == 8) { if (OZ && i == 0) WL(12); else WL(8); SBAR(); } \
        o[i & 3] = MFMA32(fr[i & 7], (i < 4 ? pk0 : i < 8 ? pk1 : i < 12 ? pk2 : pk3), (OZ && i < 4) ? f32x16{} : o[i & 3]); \
        if (i < 8) fr[i] = RDV(vb_, i + 8); \
        if (i == 10) { \
          SBAR(); WAITV(6); if (!(XC)) WAITV(4); WL(0); LBAR(T); \
          fr[0] = RDK(kb_, 0); fr[1] = RDK(kb_, 1); fr[2] = RDK(kb_, 2); \
        } \
        if (i > 10) fr[i - 8] = RDK(kb_, i - 8); \
        if (i == 11) KPIECE(0, dd_, sp_); if (i == 12) KPIECE(1, dd_, sp_); if (i == 13) KPIECE(2, dd_, sp_); if (i == 14) KPIECE(3, dd_, sp_); \
        if (i >= 1 && i <= 3) MAXG(S0, S1, i); \
        if (i == 4) { SBAR(); REFUPD(S0, S1, NEWU); } \
        if (i >= 5) { \
          constexpr int e0_[12] = {0, 1, 2, 3, 5, 7, 8, 9, 10, 11, 12, 14}; \
          _Pragma("unroll") for (int e = e0_[i - 5]; e < e0_[i - 4]; ++e) EXP1(S0, e); \
          PIN(l); } \
        if (i == 12) { pk0 = pack8<0>(S0); PIN(pk0); } \
        SBAR(); \
      } \
      if (newq) { \
        asm volatile("s_barrier" ::: "memory"); \
        _Pragma("unroll") for (int ks = 0; ks < 8; ++ks) qf[ks] = lds128(hb + ks * 1024); \
      } \
      ROT(); } while (0)
#pragma nounroll
    for (int P = 0; P < 4; ++P) {
      const int T0 = 16 * P;
      TICK_A(sa0, sa1, sb0, sb1, T0, true, false, P < 3, P + 1, 8, 4);
      TICK_B(sa0, sa1, T0 + 1, false, true, false, P < 3);
      TICK_A(sb0, sb1, sa0, sa1, T0 + 2, false, true, false, 0, 0, 0);
      {
        TICK_B(sb0, sb1, T0 + 3, false, false, true, false);
#pragma nounroll
        for (int k = 0; k < 3; ++k) {
          const int T = T0 + 4 + 4 * k;
          const int xu = P + (k == 2 ? 2 : 1);
          TICK_A(sa0, sa1, sb0, sb1, T, false, false, xu < 4, xu, k == 2 ? 1 : 4 + 2 * k, k == 2 ? 9 : 2 - 2 * k);
          TICK_B(sa0, sa1, T + 1, false, false, false, xu < 4);
          TICK_A(sb0, sb1, sa0, sa1, T + 2, false, false, P < 3 && k < 2, xu, 5 + 2 * k, 3 - 2 * k);
          TICK_B(sb0, sb1, T + 3, k == 2 && P < 3, false, false, P < 3 && k < 2);
        }
      }
    }
    {
      const unsigned vb_ = laneV + c1;
      WAITV(4); BAR(); BAR();
#define DI(i) ((((i) & 1) | (((i) & 8) >> 2) | (((i) & 6) << 1)) ^ 2)
#pragma unroll
      for (int i = 0; i < 8; ++i) fr[i] = RDV(vb_, DI(i));
      if (resc) {
#pragma unroll
        for (int e = 0; e < 64; ++e) o[e >> 4][e & 15] *= alpha;
      }
#pragma unroll
      for (int e = 14; e < 32; ++e) EXPE(sb0, sb1, e);
      pk1 = pack8<8>(sb0); pk2 = pack8<0>(sb1); pk3 = pack8<8>(sb1);
      auto r2_ = __builtin_amdgcn_permlane32_swap(F2U(l), F2U(l), false, false);
      alpha = __builtin_amdgcn_rcpf(U2F(r2_[0]) + U2F(r2_[1]));
      SBAR();
#pragma unroll
      for (int i = 0; i < 16; ++i) {
        o[DI(i) & 3] = MFMA32(fr[i & 7], ((i & 6) == 0 ? pk0 : (i & 6) == 2 ? pk1 : (i & 6) == 4 ? pk2 : pk3), o[DI(i) & 3]);
        if (i < 8) fr[i] = RDV(vb_, DI(i + 8));
        if (i >= 9 && (i & 1)) {
          const int c_ = 4 + ((i - 9) >> 1);
#pragma unroll
          for (int e = 0; e < 8; ++e) o[c_ >> 1][(c_ & 1) * 8 + e] *= alpha;
          st128(hb + c_ * 1024, (c_ & 1) ? pack8<8>(o[c_ >> 1]) : pack8<0>(o[c_ >> 1]));
        }
        SBAR();
      }
      BAR();
#pragma unroll
      for (int c_ = 0; c_ < 4; ++c_) {
#pragma unroll
        for (int e = 0; e < 8; ++e) o[c_ >> 1][(c_ & 1) * 8 + e] *= alpha;
        st128(hb + c_ * 1024, (c_ & 1) ? pack8<8>(o[c_ >> 1]) : pack8<0>(o[c_ >> 1]));
      }
      BAR();
    }
    MT(3); WAITV(0);
    STAMP_OUT();
  } else {
    const int j = wid - 4;
    f32x4 acc[9][4] = {};
    unsigned e0, e1, e2, e3;
    ASTAMP_DECL();
    {
    LANE_CONSTS();
    const char* WQ = (br ? wqB : wqA) + g * 2048 + j * 128;
    const char* WO = (br ? woB : woA) + (size_t)g * 1024 * 1152;
    const unsigned ring = L_W + j * RG;
    const unsigned voffQ = (lane >> 3) * 8192 + (((lane & 7) ^ ((g4 & 1) << 2)) * 16);
    const unsigned dvo = voffQ ^ ((lane >> 3) * 1152 + j * 256 + (((lane & 7) ^ ((g4 & 1) << 1)) * 16));
    const unsigned pos = lane >> 1;
    const unsigned voffC = (pos ^ (((pos >> 3) & 1) << 2)) * 1152 + 1024 + j * 32 + (lane & 1) * 16;
    const int dOB = (g >> 1) ? -(int)RG : (int)RG;
    f32x16 aq[2][2];
    auto isO = [](int n) -> int { return (int)(n >= 52) | (int)((unsigned)(n - 22) < 8u) | (int)((unsigned)(n - 39) < 8u); };
    auto woff = [&](int n) -> unsigned {
      const int qi = n - (n >= 30 ? 8 : 0) - (n >= 47 ? 8 : 0), u = qi / 9, s = qi - 9 * u;
      const int oi = n >= 52 ? n - 36 : n >= 39 ? n - 31 : n - 22;
      return isO(n) ? (unsigned)(((((oi >> 3) + rot) & 3) * 256 + (oi & 1) * 128 + ((oi & 6) ^ 4) * 16) * 9) | 65536u
                    : (unsigned)(xblk(r0, u, s) * 4096 + ((u + rot) & 3) * 4);
    };
    auto build = [&](int base) -> unsigned {
      const int n = base + lane_id(), m = n + 1;
      const int qi = m - (m >= 30 ? 8 : 0) - (m >= 47 ? 8 : 0), u = qi / 9, s = (qi - 9 * u) & 15;
      const int oi = m >= 52 ? m - 36 : m >= 39 ? m - 31 : m - 22;
      const unsigned boff = isO(m) ? (unsigned)((((oi & 7) >> 1) ^ 2) + (oi & 1) * 28) : (unsigned)((u == 0 ? 0x421032104ull : 0x410321094ull) >> (4 * s)) & 15u;
      const unsigned qw = n == 8 || n == 17 || n == 34 || n == 51;
      const unsigned nba = !(n == 8 || n == 11 || n == 20 || n == 37 || n >= 61);
      const unsigned last = qw || n == 21 || n == 29 || n == 38 || n == 46 || n == 67;
      return woff(n + 2 < 67 ? n + 2 : 67) | boff << 27 | nba << 21 | (unsigned)(n == 12 || n == 59) << 22 | last << 23 | qw << 24 | (unsigned)(n == 51) << 25 | (unsigned)isO(m) << 26;
    };
    u32x4 rs = {0u, 0u, 0x7fffffffu, 0x00020000u};
    unsigned soff, gstep, pstep, ve, vo; unsigned long long cmask;
    auto src = [&](unsigned D) {
      const bool io = (D & 65536u) != 0;
      const unsigned long long bp = (unsigned long long)(io ? WO : WQ);
      rs[0] = (unsigned)bp; rs[1] = (unsigned)(bp >> 32);
      soff = (D & 65535u) << 7; gstep = io ? 128u : 32u * 8192u; pstep = io ? 9216u - 1024u : 65536u - 1024u;
      cmask = io ? ~0ull : 0ull;
      const unsigned msk = io ? ~0u : 0u;
      ve = voffQ ^ (dvo & msk); vo = ve ^ (64u & msk);
      PINS(rs); PINS(soff); PINS(gstep); PINS(pstep); PINS(cmask); PIN(ve); PIN(vo);
    };
#define RF(G, c, half) do { if ((c) == 0) SETM0(ring + (half) + (G) * 4096); bdma<(c) * 1024>(rs, soff + (G) * gstep + (c) * pstep, ((c) & 1) ? vo : ve); } while (0)
#define RFC(half) asm volatile("s_mov_b32 m0, %0\n\ts_mov_b64 exec, %1\n\tbuffer_load_dwordx4 %2, %3, %4 offen lds\n\ts_mov_b64 exec, -1" ::"s"(ring + (half) + 8192), "s"(cmask), "v"(voffC), "s"(rs), "s"(soff) : "memory")
    unsigned ev;
    asm volatile("global_load_dword %0, %1, %2 sc1" : "=v"(ev) : "v"((bid ^ (lane & 3)) * 256), "s"(ws + WS_FLAG) : "memory");
    src(woff(0)); RF(0, 0, 0); RF(0, 1, 0); RF(0, 2, 0); RF(0, 3, 0); RF(1, 0, 0); RF(1, 1, 0); RF(1, 2, 0); RF(1, 3, 0);
    ASTAMP_T(1);
    unsigned tab = build(0);
    PIN(tab); ASTAMP_T(2);
    src(woff(1)); RF(0, 0, 9216); RF(0, 1, 9216); RF(0, 2, 9216); RF(0, 3, 9216);
    {
      GAS unsigned* fl = (GAS unsigned*)(ws + WS_FLAG);
      asm volatile("s_waitcnt vmcnt(12)\n\tv_add_u32 %0, 1, %0" : "+v"(ev)::"memory");
      e0 = __builtin_amdgcn_readlane(ev, 0); e1 = __builtin_amdgcn_readlane(ev, 1); e2 = __builtin_amdgcn_readlane(ev, 2); e3 = __builtin_amdgcn_readlane(ev, 3);
      ABAR();
      if (j == 0 && lane >= 1 && lane < 4) __hip_atomic_store(fl + (bid ^ lane) * 64 + 32 + lane, ev, __ATOMIC_RELAXED, __HIP_MEMORY_SCOPE_AGENT);
    }
    __builtin_amdgcn_s_setprio(2);
    unsigned half = 0, D = 4u << 27 | 1u << 24;
    int ti = 0;
#define XBAR() asm volatile("s_bitcmp0_b32 %0, 22\n\ts_cbranch_scc1 1f\n\ts_barrier\n\ts_barrier\n\ts_barrier\n\ts_barrier\n\ts_barrier\n\ts_barrier\n\ts_barrier\n\ts_barrier\n1:\n\t" \
      "s_bitcmp0_b32 %0, 21\n\ts_cbranch_scc1 2f\n\ts_barrier\n2:" ::"s"(D) : "memory", "scc")
#pragma nounroll
    for (int run = 0; run < 3; ++run) {
      {
#define QA(ks, d) tr2((aa ^ ((d) << 6)) + (ks) * 2048, (aa ^ ((d) << 6)) + (ks) * 2048 + 512)
#define QB(ks, q) lds128((bb ^ ((ks) << 5)) + (q) * 2 * RG)
#define QBB() ((laneQB & 1024u) * ((D >> 27) == 9u ? 8u : 0u) + laneQB + ((D >> 27) << 11))
#define QM1(d, q, fa, fb) do { aq[d][q] = MFMA32(fa, fb, aq[d][q]); SBAR(); } while (0)
        LANE_CONSTS();
        unsigned laneQB = L_H + (r >> 4) * RG + (r & 15) * 128 + ((h ^ ((r >> 1) & 7)) * 16);
        unsigned aa = ring + h * 1024 + q4 * 128 + (((q4 >> 1) & 1) * 64) + gi * 32 + pp * 8 + half, bb = QBB();
        bf16x8 r0_, r1, r2, x0, x1, y0, y1;
        PIN(laneQB); PIN(aa); PIN(bb); SBAR();
        WAITV(8);
        r0_ = QA(0, 0); x0 = QB(0, 0); x1 = QB(0, 1); SBAR(); r1 = QA(0, 1); SBAR(); r2 = QA(1, 0); SBAR(); y0 = QB(1, 0); y1 = QB(1, 1); SBAR();
#define QZ1(d, q, fa, fb, Z) do { aq[d][q] = MFMA32(fa, fb, Z ? f32x16{} : aq[d][q]); SBAR(); } while (0)
#define QBODY(Z) do { \
          const unsigned oh = half ^ 9216u; \
          QZ1(0, 0, r0_, x0, Z); RFC(oh); QZ1(0, 1, r0_, x1, Z); r0_ = QA(1, 1); RF(1, 0, oh); SBAR(); \
          QZ1(1, 0, r1, x0, Z); QZ1(1, 1, r1, x1, Z); RF(1, 1, oh); WAITV(6); r1 = QA(2, 0); x0 = QB(2, 0); x1 = QB(2, 1); SBAR(); \
          QM1(0, 0, r2, y0); RF(1, 2, oh); QM1(0, 1, r2, y1); r2 = QA(3, 0); SBAR(); \
          QM1(1, 0, r0_, y0); RF(1, 3, oh); QM1(1, 1, r0_, y1); r0_ = QA(2, 1); y0 = QB(3, 0); y1 = QB(3, 1); \
          D = __builtin_amdgcn_readlane(tab, ti); ++ti; src(D); SBAR(); \
          QM1(0, 0, r1, x0); RF(0, 0, half); QM1(0, 1, r1, x1); r1 = QA(3, 1); SBAR(); \
          aa += 9216u - 2 * half; PIN(laneQB); bb = QBB(); PIN(aa); PIN(bb); \
          QM1(1, 0, r0_, x0); RF(0, 1, half); QM1(1, 1, r0_, x1); \
          WL(0); WAITL0(); XBAR(); WAITV(6); \
          r0_ = QA(0, 0); x0 = QB(0, 0); x1 = QB(0, 1); SBAR(); \
          QM1(1, 0, r1, y0); RF(0, 2, half); QM1(1, 1, r1, y1); r1 = QA(0, 1); SBAR(); \
          QM1(0, 0, r2, y0); RF(0, 3, half); QM1(0, 1, r2, y1); r2 = QA(1, 0); y0 = QB(1, 0); y1 = QB(1, 1); SBAR(); \
          half ^= 9216u; } while (0)
#pragma nounroll
        do {
          if (D & (1u << 24)) QBODY(true);
#pragma nounroll
          do { QBODY(false); } while (!(D & (1u << 23)));
          if (D & (1u << 24)) {
            const unsigned d = L_H + (j >> 1) * 2 * RG + (j & 1) * 4096 + lane_id() * 16;
#pragma unroll
            for (int d2 = 1; d2 >= 0; --d2)
#pragma unroll
              for (int qt = 0; qt < 2; ++qt) {
                aq[d2][qt] *= SL2E;
                st128(d + qt * RG + d2 * 2048, pack8<0>(aq[d2][qt]));
                st128(d + qt * RG + d2 * 2048 + 1024, pack8<8>(aq[d2][qt]));
              }
            ABAR();
            y0 = QB(1, 0); y1 = QB(1, 1); SBAR();
          }
        } while (!(D & (1u << 26)));
        if (D & (1u << 25)) {
          const int l = lane_id(), o2 = (l < 13 ? l : 13) + 18, o1 = l + 17;
          tab = (unsigned)(((((o2 >> 3) + rot) & 3) * 256 + (o2 & 1) * 128 + ((o2 & 6) ^ 4) * 16) * 9) | 65536u | (unsigned)((((o1 & 7) >> 1) ^ 2) + (o1 & 1) * 28) << 27
                | (unsigned)(l <= 8) << 21 | (unsigned)(l == 7) << 22 | (unsigned)(l == 15) << 23;
          ti = 0; ABAR(); ABAR(); ABAR();
        }
      }
      {
#define OA(t) tr2((aa ^ (((t) & 3) << 5)) + ((t) >> 2) * 4096, (aa ^ (((t) & 3) << 5)) + ((t) >> 2) * 4096 + 512)
#define OM1(t, f, q) acc[t][q] = MFMA16(f, bq[q], acc[t][q])
#define OM2(t, f, q) do { OM1(t, f, q); OM1(t, f, q + 1); SBAR(); } while (0)
#define OM(t, f) do { OM2(t, f, 0); OM2(t, f, 2); } while (0)
        LANE_CONSTS();
        const unsigned laneOC = ring + 8192 + (8 * g4 + 4 * (g4 & 1) + q4) * 32 + p4 * 8;
        const unsigned laneOB = L_H + (g >> 1) * RG + (g4 >> 1) * 1024 + (g4 & 1) * 512 + (g & 1) * 256 + i16 * 16;
        unsigned aa = ring + g4 * 1024 + q4 * 128 + ((((q4 >> 1) & 1) | ((g4 & 1) << 1)) * 32) + p4 * 8 + half;
        unsigned ba = laneOB + ((D >> 27) << 11), bb = ba + dOB;
        bf16x8 bq[4], f0, f1, f2;
        WAITV(8);
        f0 = OA(0); bq[0] = lds128(ba); bq[2] = lds128(bb); bq[1] = lds128(ba ^ 256); f1 = OA(1); bq[3] = lds128(bb ^ 256); f2 = OA(2); SBAR();
#pragma nounroll
        do {
          const unsigned oh = half ^ 9216u;
          OM1(0, f0, 0); SBAR(); RFC(oh); OM1(0, f0, 2); OM1(0, f0, 1); SBAR(); RF(1, 0, oh);
          OM1(1, f1, 0); OM1(1, f1, 2); OM1(1, f1, 1); OM1(0, f0, 3); SBAR(); f0 = OA(3); RF(1, 1, oh); SBAR();
          OM1(1, f1, 3); SBAR(); WAITV(6); f1 = OA(4); SBAR();
          OM2(2, f2, 0); RF(1, 2, oh); OM2(2, f2, 2); f2 = OA(5); SBAR();
          OM2(3, f0, 0); RF(1, 3, oh); OM2(3, f0, 2); f0 = OA(6); D = __builtin_amdgcn_readlane(tab, ti); ++ti; src(D); SBAR();
          OM2(4, f1, 0); RF(0, 0, half); OM2(4, f1, 2); f1 = OA(7); SBAR();
          OM2(5, f2, 0); RF(0, 1, half); OM2(5, f2, 2); { const unsigned ac = laneOC + half; f2 = tr2(ac, ac ^ 128); } SBAR();
          aa += 9216u - 2 * half; ba = laneOB + ((D >> 27) << 11); bb = ba + dOB; PIN(aa); PIN(ba); PIN(bb);
          OM2(6, f0, 0); RF(0, 2, half); OM2(6, f0, 2);
          OM1(7, f1, 0); OM1(7, f1, 2); OM1(8, f2, 0); OM1(8, f2, 2); SBAR();
          WL(0); WAITL0(); XBAR(); WAITV(7);
          f0 = OA(0); bq[0] = lds128(ba); bq[2] = lds128(bb); SBAR();
          OM1(7, f1, 1); SBAR(); RF(0, 3, half); OM1(8, f2, 1); SBAR(); bq[1] = lds128(ba ^ 256); SBAR();
          OM1(7, f1, 3); SBAR(); f1 = OA(1); OM1(8, f2, 3); SBAR(); bq[3] = lds128(bb ^ 256); f2 = OA(2); SBAR();
          half ^= 9216u;
        } while (!(D & (1u << 23)));
      }
    }
    }
    LANE_CONSTS();
    ASTAMP_T(3);
    const __amdgpu_buffer_rsrc_t rs = __builtin_amdgcn_make_buffer_rsrc((void*)(ws + WS_SLAB), 0, (int)WS_SLAB_BYTES, 0x00020000);
#pragma unroll
    for (int i = 1; i < 4; ++i) {
      const int so = (((bid ^ i) * 3 + (i - 1)) * 4 + j) * 4608;
#pragma unroll
      for (int t = 0; t < 8; t += 2) {
        const u32x4 v = {cvtpk(acc[t][i][0], acc[t][i][1]), cvtpk(acc[t][i][2], acc[t][i][3]), cvtpk(acc[t + 1][i][0], acc[t + 1][i][1]), cvtpk(acc[t + 1][i][2], acc[t + 1][i][3])};
        __builtin_amdgcn_raw_buffer_store_b128(v, rs, lane * 16 + t * 512, so, 16);
      }
      const u32x2 v = {cvtpk(acc[8][i][0], acc[8][i][1]), cvtpk(acc[8][i][2], acc[8][i][3])};
      __builtin_amdgcn_raw_buffer_store_b64(v, rs, lane * 8 + 4096, so, 16);
    }
    GAS unsigned* fl = (GAS unsigned*)(ws + WS_FLAG);
    const unsigned want = (lane & 3) == 0 ? e0 : (lane & 3) == 1 ? e1 : (lane & 3) == 2 ? e2 : e3;
    if (j == 0) {
      for (unsigned spins = 0; spins < (1u << 20); ++spins) {
        unsigned v = e0;
        if (lane >= 1 && lane < 4) v = __hip_atomic_load(fl + bid * 64 + 32 + lane, __ATOMIC_RELAXED, __HIP_MEMORY_SCOPE_AGENT);
        if (ALL(v == e0)) break;
        __builtin_amdgcn_s_sleep(1);
      }
    }
    WAITV(0);
    BAR();
    ASTAMP_T(4);
    if (j == 0 && lane == 0) __hip_atomic_store(fl + bid * 64, e0, __ATOMIC_RELAXED, __HIP_MEMORY_SCOPE_AGENT);
    for (unsigned spins = 0; spins < (1u << 20); ++spins) {
      unsigned v = want;
      if (lane >= 1 && lane < 4) v = __hip_atomic_load(fl + (bid ^ lane) * 64, __ATOMIC_RELAXED, __HIP_MEMORY_SCOPE_AGENT);
      if (ALL(v == want)) break;
      __builtin_amdgcn_s_sleep(1);
    }
    __builtin_amdgcn_fence(__ATOMIC_ACQUIRE, "wavefront");
    ASTAMP_T(5);
    u32x4 pv[3][4]; u32x2 pw[3];
#pragma unroll
    for (int i = 1; i < 4; ++i) {
      const int so = ((bid * 3 + (i - 1)) * 4 + j) * 4608;
#pragma unroll
      for (int t = 0; t < 4; ++t) pv[i - 1][t] = __builtin_amdgcn_raw_buffer_load_b128(rs, lane * 16 + t * 1024, so, 16);
      pw[i - 1] = __builtin_amdgcn_raw_buffer_load_b64(rs, lane * 8 + 4096, so, 16);
    }
    char* orow = out + ((size_t)(b * 64 + 16 * g + i16) * 1152 + br * 576) * 2;
    unsigned c0_ = 0x00003f80u, c1_ = 0x3f800000u; PIN(c0_); PIN(c1_);
    const bf16x2 sel0 = __builtin_bit_cast(bf16x2, c0_), sel1 = __builtin_bit_cast(bf16x2, c1_);
    u32x2 pk[9];
#pragma unroll
    for (int t = 0; t < 9; ++t) {
      f32x4 v = acc[t][0];
#pragma unroll
      for (int i = 0; i < 3; ++i) {
        const bf16x2 lo = __builtin_bit_cast(bf16x2, t < 8 ? pv[i][t >> 1][(t & 1) * 2] : pw[i][0]), hi = __builtin_bit_cast(bf16x2, t < 8 ? pv[i][t >> 1][(t & 1) * 2 + 1] : pw[i][1]);
        v[0] = __builtin_amdgcn_fdot2_f32_bf16(lo, sel0, v[0], false); v[1] = __builtin_amdgcn_fdot2_f32_bf16(lo, sel1, v[1], false);
        v[2] = __builtin_amdgcn_fdot2_f32_bf16(hi, sel0, v[2], false); v[3] = __builtin_amdgcn_fdot2_f32_bf16(hi, sel1, v[3], false);
      }
      pk[t] = (u32x2){cvtpk(v[0], v[1]), cvtpk(v[2], v[3])};
      if (t & 1) {
        auto s0 = __builtin_amdgcn_permlane16_swap(pk[t - 1][0], pk[t][0], false, false);
        auto s1 = __builtin_amdgcn_permlane16_swap(pk[t - 1][1], pk[t][1], false, false);
        *(GAS u32x4*)(orow + (128 * j + 16 * (t - 1) + 16 * (g4 & 1) + 8 * (g4 >> 1)) * 2) = (u32x4){s0[0], s1[0], s0[1], s1[1]};
      }
    }
    *(GAS u32x2*)(orow + (512 + 16 * j + 4 * g4) * 2) = pk[8];
    ASTAMP_OUT();
  }
}

#ifdef __HIP_DEVICE_COMPILE__
#pragma clang attribute pop
#endif
extern "C" void kernel_launch(void* const* d_in, const int* in_sizes, int n_in, void* d_out, int out_size, void* d_ws, size_t ws_size,
                              hipStream_t stream) {
  (void)in_sizes; (void)n_in; (void)out_size; (void)ws_size;
  (void)hipFuncSetAttribute((const void*)fused_gqa, hipFuncAttributeMaxDynamicSharedMemorySize, (int)L_TOT);
  hipLaunchKernelGGL(fused_gqa, dim3(256), dim3(512), L_TOT, stream, (const char*)d_in[0], (const char*)d_in[1], (const char*)d_in[2],
                     (const char*)d_in[3], (const char*)d_in[4], (const char*)d_in[5], (const char*)d_in[6], (const char*)d_in[7],
                     (const char*)d_in[8], (const char*)d_in[9], (char*)d_out, (char*)d_ws);
}
```
